```python
import torch

def _softmax(x, axis=-1):
    return torch.softmax(x, dim=axis)
def _gelu(x):
    return torch.nn.functional.gelu(x, approximate="tanh")
def _silu(x):
    return torch.nn.functional.silu(x)
def _sigmoid(x):
    return torch.sigmoid(x)
def _layernorm(x, axis=-1, eps=1e-5):
    m = x.mean(dim=axis, keepdim=True)
    v = x.var(dim=axis, keepdim=True, unbiased=False)
    return (x - m) / torch.sqrt(v + eps)
def _scatter_add(tgt, idx, src, axis=0):
    return tgt.clone().index_add_(axis, idx, src)
def _segment_sum(src, seg, n):
    out = torch.zeros((n,) + src.shape[1:], dtype=src.dtype)
    return out.index_add_(0, seg, src)
def _pad(x, pw):
    flat = tuple(v for pair in reversed(pw) for v in pair)
    return torch.nn.functional.pad(x, flat)

kernel_name = 'sequential_preLN__gqa_block_cached__mixer_token_mlp__decode'

def setup_inputs():

    _w = (lambda a: (lambda t: t.to(torch.bfloat16) if t.is_floating_point() else t)(torch.as_tensor(a)))
    def _A_setup():
        B, Sq, D = 2, 1, 640
        Hq, Hkv, Sk, Dh = 32, 32, 8192, 192
        assert Hq % Hkv == 0, (Hq, Hkv)
        return {
            "x": _w((lambda *s: torch.randn(*s, dtype=torch.bfloat16) * 0.05)(B, Sq, D)),
            "k_cache": _w((lambda *s: torch.randn(*s, dtype=torch.bfloat16) * 1.6)(B, Hkv, Sk, Dh)),
            "v_cache": _w((lambda *s: torch.randn(*s, dtype=torch.bfloat16) * 0.8)(B, Hkv, Sk, Dh)),
            "wq": _w((lambda *s: torch.randn(*s, dtype=torch.bfloat16) * 1.6)(D, Hq * Dh) * (D ** -0.5)),
            "wo": _w((lambda *s: torch.randn(*s, dtype=torch.bfloat16) * 0.8)(Hq * Dh, D) * ((Hq * Dh) ** -0.5)),
        }
    _A_in = _A_setup()

    def _B_setup():
        B, S, D, F = 2, 1, 640, 1536
        return {
            "x": _w((lambda *s: torch.randn(*s, dtype=torch.bfloat16) * 0.05)(B, S, D)),
            "w1": _w((lambda *s: torch.randn(*s, dtype=torch.bfloat16) * 1.6)(S, F) * (S ** -0.5)),
            "b1": _w((lambda *s: torch.randn(*s, dtype=torch.bfloat16) * 1.6)(F) * 0.02),
            "w2": _w((lambda *s: torch.randn(*s, dtype=torch.bfloat16) * 1.6)(F, S) * (F ** -0.5)),
            "b2": _w((lambda *s: torch.randn(*s, dtype=torch.bfloat16) * 0.8)(S) * 0.02),
        }
    _B_in = _B_setup()

    return {
        "A_x": _A_in["x"],
        "A_k_cache": _A_in["k_cache"],
        "A_v_cache": _A_in["v_cache"],
        "A_wq": _A_in["wq"],
        "A_wo": _A_in["wo"],
        "B_x": _B_in["x"],
        "B_w1": _B_in["w1"],
        "B_b1": _B_in["b1"],
        "B_w2": _B_in["w2"],
        "B_b2": _B_in["b2"],
    }

def reference(A_x, A_k_cache, A_v_cache, A_wq, A_wo, B_x, B_w1, B_b1, B_w2, B_b2):
    def _A_ref(x, k_cache, v_cache, wq, wo):
        B, Sq, D = x.shape
        Hq, Dh = 32, 192
        Hkv = k_cache.shape[1]
        rep = Hq // Hkv
        q = (lambda x, *d: x.permute(*d))((x @ wq).reshape(B, Sq, Hq, Dh), 0, 2, 1, 3)
        k = (lambda x, n, axis: torch.repeat_interleave(x, n, dim=axis))(k_cache, rep, axis=1)
        v = (lambda x, n, axis: torch.repeat_interleave(x, n, dim=axis))(v_cache, rep, axis=1)
        scores = torch.einsum("bhqd,bhkd->bhqk", q, k) * (Dh ** -0.5)
        p = _softmax(scores, axis=-1)
        o = torch.einsum("bhqk,bhkd->bhqd", p, v)
        return (lambda x, *d: x.permute(*d))(o, 0, 2, 1, 3).reshape(B, Sq, Hq * Dh) @ wo

    def _B_ref(x, w1, b1, w2, b2):
        B, S, D = x.shape
        h = _layernorm(x, axis=-1)
        u = torch.einsum("bsd,sf->bdf", h, w1) + b1
        u = _gelu(u)
        y = torch.einsum("bdf,fs->bds", u, w2) + b2
        return (lambda x, *d: x.permute(*d))(y, 0, 2, 1)

    x = A_x
    h = x + _A_ref(_layernorm(x, axis=-1), A_k_cache, A_v_cache, A_wq, A_wo)
    h2 = h + _B_ref(_layernorm(h, axis=-1), B_w1, B_b1, B_w2, B_b2)
    return h2
```

```cpp
#include <hip/hip_runtime.h>
#include <hip/hip_bf16.h>
#include <stdint.h>

namespace {

typedef unsigned u32x4 __attribute__((ext_vector_type(4)));
typedef unsigned u32x2 __attribute__((ext_vector_type(2)));
typedef float f32x2 __attribute__((ext_vector_type(2)));
typedef __bf16 bf16x2 __attribute__((ext_vector_type(2)));

constexpr int D = 640, H = 32, SK = 8192, DH = 192, FF = 1536;
constexpr int NWG = 256, NTHR = 512, NWAVE = 8;
constexpr int KEYS_WG = SK / 4;
constexpr int TILES_WG = KEYS_WG / 8;
constexpr int TILE_BYTES = 8 * DH * 2;
#define NSW 4
constexpr int AUX_NT = 2, AUX_SC1 = 16;
constexpr unsigned RSRC3 = 0x00020000u;
constexpr unsigned OOB = 0x80000000u;

constexpr unsigned WS_EPOCH = 0;
constexpr unsigned WS_P = 4096;
constexpr unsigned WS_Y = WS_P + NWG * 2048;
constexpr unsigned WS_END = WS_Y + 2 * 8 * 320 * 16;

constexpr int L_RED = 0;
constexpr int L_ML = L_RED + NWAVE * DH;
constexpr int L_Q = L_ML + 16;
constexpr int L_O = L_Q + 96;
constexpr int L_YR = L_O + 384;
constexpr int L_H = L_YR + 320;
constexpr int L_MR = L_H + 640;
constexpr int L_CNT = L_MR + 64;
constexpr int L_USED = L_CNT + 4;
constexpr int L_TOTAL = 24 * 1024;
static_assert(L_USED <= L_TOTAL, "lds");

__device__ __forceinline__ float dot2(unsigned a, unsigned b, float c) {
  return __builtin_amdgcn_fdot2_f32_bf16(__builtin_bit_cast(bf16x2, a), __builtin_bit_cast(bf16x2, b), c, false);
}
template <int CTRL> __device__ __forceinline__ float dpp(float x) {
  return __builtin_bit_cast(float, __builtin_amdgcn_mov_dpp(__builtin_bit_cast(int, x), CTRL, 0xf, 0xf, true));
}
constexpr int XOR1 = 0xB1, XOR2 = 0x4E, HMIR = 0x141, ROR8 = 0x128;
__device__ __forceinline__ float bflo(unsigned u) { return __builtin_bit_cast(float, u << 16); }
__device__ __forceinline__ float bfhi(unsigned u) { return __builtin_bit_cast(float, u & 0xffff0000u); }
__device__ __forceinline__ unsigned pack_bf16(float lo, float hi) {
  bf16x2 v; v[0] = (__bf16)lo; v[1] = (__bf16)hi;
  return __builtin_bit_cast(unsigned, v);
}
__device__ __forceinline__ float rbf(float x) { return (float)(__bf16)x; }
__device__ __forceinline__ float fold32(float a, float b) {
  auto r = __builtin_amdgcn_permlane32_swap(__float_as_uint(a), __float_as_uint(b), false, false);
  return __uint_as_float(r[0]) + __uint_as_float(r[1]);
}
__device__ __forceinline__ float fold16(float a, float b) {
  auto r = __builtin_amdgcn_permlane16_swap(__float_as_uint(a), __float_as_uint(b), false, false);
  return __uint_as_float(r[0]) + __uint_as_float(r[1]);
}
__device__ __forceinline__ float max3216(float x) {
  auto s = __builtin_amdgcn_permlane16_swap(__float_as_uint(x), __float_as_uint(x), false, false);
  x = fmaxf(__uint_as_float(s[0]), __uint_as_float(s[1]));
  auto t = __builtin_amdgcn_permlane32_swap(__float_as_uint(x), __float_as_uint(x), false, false);
  return fmaxf(__uint_as_float(t[0]), __uint_as_float(t[1]));
}
__device__ __forceinline__ float wave_sum(float x) {
  x += dpp<XOR1>(x); x += dpp<XOR2>(x); x += dpp<HMIR>(x); x += dpp<ROR8>(x);
  x = fold16(x, x); x = fold32(x, x);
  return x;
}
__device__ __forceinline__ unsigned long long mix64(unsigned long long z) {
  z += 0x9E3779B97F4A7C15ull;
  z = (z ^ (z >> 30)) * 0xBF58476D1CE4E5B9ull;
  z = (z ^ (z >> 27)) * 0x94D049BB133111EBull;
  return z ^ (z >> 31);
}
__device__ __forceinline__ u32x4 pack2(float a, float b, unsigned t8, unsigned thi) {
  u32x4 r; r.x = (__float_as_uint(a) >> 8) | t8; r.y = thi; r.z = (__float_as_uint(b) >> 8) | t8; r.w = thi; return r;
}
__device__ __forceinline__ unsigned tag_bad(u32x4 g, unsigned t8, unsigned thi) {
  return (((g.x ^ t8) | (g.z ^ t8)) >> 24) | (g.y ^ thi) | (g.w ^ thi);
}
__device__ __forceinline__ float g0(u32x4 g) { return __uint_as_float(g.x << 8); }
__device__ __forceinline__ float g1(u32x4 g) { return __uint_as_float(g.z << 8); }
__device__ __forceinline__ float trunc24(float x) { return __uint_as_float(__float_as_uint(x) & 0xffffff00u); }

#define SB() __builtin_amdgcn_sched_barrier(0)
#define LD128(rs, vo, so, aux) __builtin_amdgcn_raw_buffer_load_b128((rs), (vo), (so), (aux))
#define STAMP(i) do { } while (0)

constexpr unsigned SPIN_CAP = 1u << 16;

__device__ __forceinline__ void reduce24_store(float (&a)[24], float* dst, int lane) {
  float z[12];
#pragma unroll
  for (int t = 0; t < 3; ++t)
#pragma unroll
    for (int j = 0; j < 4; ++j) z[t * 4 + j] = fold32(a[t * 8 + j], a[t * 8 + j + 4]);
  const int c = lane & 7, hi = lane >> 5, odd = (lane >> 4) & 1;
#pragma unroll
  for (int t = 0; t < 3; ++t) {
    float y0 = fold16(z[t * 4 + 0], z[t * 4 + 2]);
    float y1 = fold16(z[t * 4 + 1], z[t * 4 + 3]);
    y0 += dpp<ROR8>(y0); y1 += dpp<ROR8>(y1);
    if ((lane & 8) == 0) *reinterpret_cast<f32x2*>(dst + 64 * t + 8 * c + 4 * hi + 2 * odd) = f32x2{y0, y1};
  }
}

__global__ void __launch_bounds__(NTHR, 2)
fused_kernel(const unsigned* x, const char* kc, const char* vc,
             const char* wq, const char* wo, const unsigned short* w1,
             const unsigned short* b1, const unsigned short* w2, const unsigned short* b2,
             __hip_bfloat16* out, char* ws) {
  __shared__ __attribute__((aligned(16))) float lds[L_TOTAL];
  const int tid = threadIdx.x, lane = tid & 63, wave = __builtin_amdgcn_readfirstlane(tid >> 6);
  const int bid = blockIdx.x, team = bid & 7, jj = bid >> 3, hl = jj >> 3, b = (jj >> 2) & 1, sp = jj & 3;
  const int h = team * 4 + hl, cb = hl * 4 + sp;
  const int r = lane >> 3, c = lane & 7;
  STAMP(0);
  asm volatile("" : "+s"(x), "+s"(kc), "+s"(vc), "+s"(wq), "+s"(wo), "+s"(w1), "+s"(b1), "+s"(w2), "+s"(b2), "+s"(out), "+s"(ws));

  const size_t kvbase = ((size_t)(b * H + h) * SK + (size_t)sp * KEYS_WG) * (DH * 2);
  const __amdgpu_buffer_rsrc_t rk = __builtin_amdgcn_make_buffer_rsrc((void*)(kc + kvbase), 0, KEYS_WG * DH * 2, RSRC3);
  const __amdgpu_buffer_rsrc_t rv = __builtin_amdgcn_make_buffer_rsrc((void*)(vc + kvbase), 0, KEYS_WG * DH * 2, RSRC3);
  const __amdgpu_buffer_rsrc_t rq = __builtin_amdgcn_make_buffer_rsrc((void*)wq, 0, D * H * DH * 2, RSRC3);
  const __amdgpu_buffer_rsrc_t ro = __builtin_amdgcn_make_buffer_rsrc((void*)wo, 0, D * H * DH * 2, RSRC3);
  const __amdgpu_buffer_rsrc_t rw = __builtin_amdgcn_make_buffer_rsrc((void*)ws, 0, WS_END, RSRC3);
  const __amdgpu_buffer_rsrc_t rx = __builtin_amdgcn_make_buffer_rsrc((void*)x, 0, 2 * D * 2, RSRC3);

  if (tid == 0) reinterpret_cast<unsigned*>(lds)[L_CNT] = 2 * NWAVE;

  unsigned vzero;
  asm volatile("v_mov_b32 %0, 0" : "=v"(vzero));
  const u32x2 ep = __builtin_amdgcn_raw_buffer_load_b64(rw, vzero, WS_EPOCH + b * 256, AUX_SC1);
  unsigned xs[5], xp[5];
#pragma unroll
  for (int i = 0; i < 5; ++i) xs[i] = __builtin_amdgcn_raw_buffer_load_b32(rx, lane * 4 + i * 256, b * D * 2, 0);
#pragma unroll
  for (int i = 0; i < 5; ++i) xp[i] = __builtin_amdgcn_raw_buffer_load_b32(rx, r * 4 + i * 32, b * D * 2 + wave * 160, 0);
  SB();
  const unsigned kvo = r * (DH * 2) + c * 16;
  u32x4 ka[3], va[3], kb[3], vb[3];
#pragma unroll
  for (int t = 0; t < 3; ++t) { ka[t] = LD128(rk, kvo + t * 128, wave * TILE_BYTES, AUX_NT); SB(); }
#pragma unroll
  for (int t = 0; t < 3; ++t) { va[t] = LD128(rv, kvo + t * 128, wave * TILE_BYTES, AUX_NT); SB(); }
  u32x4 wqr[5][2][3];
  {
    const unsigned vo = (unsigned)(2 * r) * (H * DH * 2) + c * 16;
    const unsigned so = (unsigned)(80 * wave) * (H * DH * 2) + h * (DH * 2);
#pragma unroll
    for (int i = 0; i < 5; ++i)
#pragma unroll
      for (int e = 0; e < 2; ++e)
#pragma unroll
        for (int t = 0; t < 3; ++t) { wqr[i][e][t] = LD128(rq, vo + t * 128, so + (16 * i + e) * (H * DH * 2), 0); SB(); }
  }
#pragma unroll
  for (int t = 0; t < 3; ++t) { kb[t] = LD128(rk, kvo + t * 128, (NWAVE + wave) * TILE_BYTES, AUX_NT); SB(); }
#pragma unroll
  for (int t = 0; t < 3; ++t) { vb[t] = LD128(rv, kvo + t * 128, (NWAVE + wave) * TILE_BYTES, AUX_NT); SB(); }

  unsigned xn[5];
  {
    float s = 0.f;
#pragma unroll
    for (int i = 0; i < 5; ++i) s += bflo(xs[i]) + bfhi(xs[i]);
    const float mean = wave_sum(s) * (1.0f / D);
    float v = 0.f;
#pragma unroll
    for (int i = 0; i < 5; ++i) { const float a0 = bflo(xs[i]) - mean, a1 = bfhi(xs[i]) - mean; v += a0 * a0 + a1 * a1; }
    const float var = wave_sum(v) * (1.0f / D);
    const float rs = __builtin_amdgcn_rsqf(var + 1e-5f);
#pragma unroll
    for (int i = 0; i < 5; ++i) xn[i] = pack_bf16((bflo(xp[i]) - mean) * rs, (bfhi(xp[i]) - mean) * rs);
  }
  {
    float aq[24];
#pragma unroll
    for (int k = 0; k < 24; ++k) aq[k] = 0.f;
#pragma unroll
    for (int i = 0; i < 5; ++i)
#pragma unroll
      for (int t = 0; t < 3; ++t)
#pragma unroll
        for (int j = 0; j < 4; ++j) {
          const unsigned a0 = wqr[i][0][t][j], a1 = wqr[i][1][t][j];
          aq[t * 8 + 2 * j] = dot2(__builtin_amdgcn_perm(a1, a0, 0x05040100u), xn[i], aq[t * 8 + 2 * j]);
          aq[t * 8 + 2 * j + 1] = dot2(__builtin_amdgcn_perm(a1, a0, 0x07060302u), xn[i], aq[t * 8 + 2 * j + 1]);
        }
    reduce24_store(aq, lds + L_RED + wave * DH, lane);
  }
  __syncthreads();
  if (tid < 96) {
    float q0 = 0.f, q1 = 0.f;
#pragma unroll
    for (int w = 0; w < NWAVE; ++w) { const f32x2 v = *reinterpret_cast<const f32x2*>(lds + L_RED + w * DH + 2 * tid); q0 += v.x; q1 += v.y; }
    reinterpret_cast<unsigned*>(lds)[L_Q + tid] = pack_bf16(q0, q1);
  }
  __syncthreads();
  u32x4 q[3];
#pragma unroll
  for (int t = 0; t < 3; ++t) q[t] = *reinterpret_cast<const u32x4*>(lds + L_Q + (8 * t + c) * 4);
  unsigned long long tag = mix64(((unsigned long long)(unsigned)__builtin_amdgcn_readfirstlane(ep.y) << 32) | (unsigned)__builtin_amdgcn_readfirstlane(ep.x));
  asm volatile("" : "+s"(tag));
  const unsigned thi = (unsigned)(tag >> 32), t8 = (unsigned)tag & 0xff000000u;
  STAMP(1);

  constexpr float C2 = 0.07216878364870322f * 1.4426950408889634f;
  float m = -INFINITY, l = 0.f, acc[24];
#pragma unroll
  for (int k = 0; k < 24; ++k) acc[k] = 0.f;

#define SCORE(KB, S) { \
    float s0_ = 0.f, s1_ = 0.f, s2_ = 0.f; \
    _Pragma("unroll") for (int j = 0; j < 4; ++j) { s0_ = dot2(KB[0][j], q[0][j], s0_); s1_ = dot2(KB[1][j], q[1][j], s1_); s2_ = dot2(KB[2][j], q[2][j], s2_); } \
    S = s0_ + s1_ + s2_; }
#define SOFTMAX_PV(VB, S) { \
    float s_ = S; s_ += dpp<XOR1>(s_); s_ += dpp<XOR2>(s_); s_ += dpp<HMIR>(s_); s_ *= C2; \
    const float mn_ = fmaxf(m, s_); \
    const float al_ = __builtin_amdgcn_exp2f(m - mn_), p_ = __builtin_amdgcn_exp2f(s_ - mn_); \
    m = mn_; l = fmaf(l, al_, p_); \
    _Pragma("unroll") for (int t = 0; t < 3; ++t) \
      _Pragma("unroll") for (int j = 0; j < 4; ++j) { \
        acc[t * 8 + 2 * j] = fmaf(p_, bflo(VB[t][j]), acc[t * 8 + 2 * j] * al_); \
        acc[t * 8 + 2 * j + 1] = fmaf(p_, bfhi(VB[t][j]), acc[t * 8 + 2 * j + 1] * al_); } }
#define PIN_ACC() asm volatile("" : "+v"(acc[0]), "+v"(acc[1]), "+v"(acc[2]), "+v"(acc[3]), "+v"(acc[4]), "+v"(acc[5]), "+v"(acc[6]), "+v"(acc[7]), \
    "+v"(acc[8]), "+v"(acc[9]), "+v"(acc[10]), "+v"(acc[11]), "+v"(acc[12]), "+v"(acc[13]), "+v"(acc[14]), "+v"(acc[15]), \
    "+v"(acc[16]), "+v"(acc[17]), "+v"(acc[18]), "+v"(acc[19]), "+v"(acc[20]), "+v"(acc[21]), "+v"(acc[22]), "+v"(acc[23]) :: "memory")

  unsigned* const cnt = reinterpret_cast<unsigned*>(lds) + L_CNT;
#define GRAB(T) { unsigned nx_ = 0; if (lane == 0) nx_ = __hip_atomic_fetch_add(cnt, 1u, __ATOMIC_RELAXED, __HIP_MEMORY_SCOPE_WORKGROUP); \
    T = __builtin_amdgcn_readfirstlane(nx_); }
#define STEP_A(T) { \
    const unsigned so_ = T < TILES_WG ? (unsigned)T * TILE_BYTES : OOB; \
    float sc_; \
    SCORE(ka, sc_); \
    asm volatile("" : "+v"(sc_) :: "memory"); \
    SB(); \
    _Pragma("unroll") for (int u = 0; u < 3; ++u) ka[u] = LD128(rk, kvo + u * 128, so_, AUX_NT); \
    SB(); \
    SOFTMAX_PV(va, sc_); \
    PIN_ACC(); \
    SB(); \
    _Pragma("unroll") for (int u = 0; u < 3; ++u) va[u] = LD128(rv, kvo + u * 128, so_, AUX_NT); \
    SB(); }
  {
    int t = TILES_WG;
    if (wave < NSW) GRAB(t);
    STEP_A(t);
    {
      float sb;
      SCORE(kb, sb);
      SOFTMAX_PV(vb, sb);
      PIN_ACC();
      SB();
    }
    while (t < TILES_WG) {
      GRAB(t);
      STEP_A(t);
    }
    if (wave >= NSW) {
      unsigned spins = 0;
#pragma nounroll
      while (__hip_atomic_load(cnt, __ATOMIC_RELAXED, __HIP_MEMORY_SCOPE_WORKGROUP) < (unsigned)TILES_WG && ++spins < (1u << 20))
        __builtin_amdgcn_s_sleep(8);
    }
  }
  u32x4 wor[6][2];
  {
    const unsigned vo = c < 5 ? (unsigned)(2 * r) * (D * 2) + c * 16 : OOB;
    const unsigned so = (unsigned)(768 * team + 96 * wave) * (D * 2) + cb * 80;
#pragma unroll
    for (int i = 0; i < 6; ++i)
#pragma unroll
      for (int e = 0; e < 2; ++e) wor[i][e] = LD128(ro, vo, so + (16 * i + e) * (D * 2), 0);
  }
  unsigned short mw1[3], mb1[3], mw2[3];
  {
    const __amdgpu_buffer_rsrc_t r1 = __builtin_amdgcn_make_buffer_rsrc((void*)w1, 0, FF * 2 + 4, RSRC3);
    const __amdgpu_buffer_rsrc_t r2 = __builtin_amdgcn_make_buffer_rsrc((void*)b1, 0, FF * 2 + 4, RSRC3);
    const __amdgpu_buffer_rsrc_t r3 = __builtin_amdgcn_make_buffer_rsrc((void*)w2, 0, FF * 2 + 4, RSRC3);
#pragma unroll
    for (int k = 0; k < 3; ++k) {
      mw1[k] = __builtin_amdgcn_raw_buffer_load_b16(r1, tid * 2 + 1024 * k, 0, 0);
      mb1[k] = __builtin_amdgcn_raw_buffer_load_b16(r2, tid * 2 + 1024 * k, 0, 0);
      mw2[k] = __builtin_amdgcn_raw_buffer_load_b16(r3, tid * 2 + 1024 * k, 0, 0);
    }
  }
  const unsigned short mb2 = __builtin_amdgcn_raw_buffer_load_b16(__builtin_amdgcn_make_buffer_rsrc((void*)b2, 0, 4, RSRC3), vzero, 0, 0);
  const unsigned xres = __builtin_amdgcn_raw_buffer_load_b32(rx, tid < 320 ? tid * 4 : OOB, b * D * 2, 0);
  SB();
  STAMP(2);

  {
    float mw = fmaxf(m, dpp<ROR8>(m));
    mw = max3216(mw);
    const float f = __builtin_amdgcn_exp2f(m - mw);
#pragma unroll
    for (int k = 0; k < 24; ++k) acc[k] *= f;
    float lw = l * f;
    lw += dpp<ROR8>(lw); lw = fold16(lw, lw); lw = fold32(lw, lw);
    reduce24_store(acc, lds + L_RED + wave * DH, lane);
    if (lane == 0) *reinterpret_cast<f32x2*>(lds + L_ML + 2 * wave) = f32x2{mw, lw};
  }
  __syncthreads();
  if (tid < 97) {
    float M = -INFINITY;
#pragma unroll
    for (int w = 0; w < NWAVE; ++w) M = fmaxf(M, lds[L_ML + 2 * w]);
    M = trunc24(M);
    float L = 0.f, o0 = 0.f, o1 = 0.f;
    const int d2 = tid < 96 ? 2 * tid : 0;
#pragma unroll
    for (int w = 0; w < NWAVE; ++w) {
      const f32x2 ml = *reinterpret_cast<const f32x2*>(lds + L_ML + 2 * w);
      const f32x2 v = *reinterpret_cast<const f32x2*>(lds + L_RED + w * DH + d2);
      const float f = __builtin_amdgcn_exp2f(ml.x - M);
      L = fmaf(f, ml.y, L); o0 = fmaf(f, v.x, o0); o1 = fmaf(f, v.y, o1);
    }
    if (tid == 96) { o0 = M; o1 = L; }
    __builtin_amdgcn_raw_buffer_store_b128(pack2(o0, o1, t8, thi), rw, tid * 16, WS_P + bid * 2048, AUX_SC1);
  }
  STAMP(3);

  const int hh = tid / 96, dp = tid - hh * 96;
  const unsigned pbase = wave < 6 ? WS_P + (unsigned)(team + 8 * (hh * 8 + b * 4)) * 2048 : OOB;
  u32x4 go[4], gm[4];
#define POLL1() _Pragma("unroll") for (int s = 0; s < 4; ++s) { \
    go[s] = LD128(rw, pbase + dp * 16, s * 16384, AUX_SC1); gm[s] = LD128(rw, pbase + 96 * 16, s * 16384, AUX_SC1); }
  POLL1();
  SB();
  unsigned wop[6][8];
#pragma unroll
  for (int i = 0; i < 6; ++i) {
#pragma unroll
    for (int j = 0; j < 4; ++j) {
      wop[i][2 * j] = __builtin_amdgcn_perm(wor[i][1][j], wor[i][0][j], 0x05040100u);
      wop[i][2 * j + 1] = __builtin_amdgcn_perm(wor[i][1][j], wor[i][0][j], 0x07060302u);
    }
    asm volatile("" : "+v"(wop[i][0]), "+v"(wop[i][1]), "+v"(wop[i][2]), "+v"(wop[i][3]), "+v"(wop[i][4]), "+v"(wop[i][5]), "+v"(wop[i][6]), "+v"(wop[i][7]) :: "memory");
  }
  SB();
  if (wave < 6) {
    for (unsigned spins = 0;; ++spins) {
      unsigned bad = 0;
#pragma unroll
      for (int s = 0; s < 4; ++s) bad |= tag_bad(go[s], t8, thi) | tag_bad(gm[s], t8, thi);
      if (__all(bad == 0u) || spins > SPIN_CAP) break;
      __builtin_amdgcn_s_sleep(2);
      asm volatile("" ::: "memory");
      POLL1();
    }
    float M = fmaxf(fmaxf(g0(gm[0]), g0(gm[1])), fmaxf(g0(gm[2]), g0(gm[3])));
    float L = 0.f, o0 = 0.f, o1 = 0.f;
#pragma unroll
    for (int s = 0; s < 4; ++s) {
      const float f = __builtin_amdgcn_exp2f(g0(gm[s]) - M);
      L = fmaf(f, g1(gm[s]), L); o0 = fmaf(f, g0(go[s]), o0); o1 = fmaf(f, g1(go[s]), o1);
    }
    const float inv = __builtin_amdgcn_rcpf(L);
    reinterpret_cast<unsigned*>(lds)[L_O + tid] = pack_bf16(o0 * inv, o1 * inv);
  }
  __syncthreads();
  STAMP(4);
  {
    float y[8];
#pragma unroll
    for (int k = 0; k < 8; ++k) y[k] = 0.f;
#pragma unroll
    for (int i = 0; i < 6; ++i) {
      const unsigned op = reinterpret_cast<const unsigned*>(lds)[L_O + 48 * wave + 8 * i + r];
#pragma unroll
      for (int k = 0; k < 8; ++k) y[k] = dot2(wop[i][k], op, y[k]);
    }
    float z0 = fold32(y[0], y[4]), z1 = fold32(y[1], y[5]), z2 = fold32(y[2], y[6]), z3 = fold32(y[3], y[7]);
    float u0 = fold16(z0, z2), u1 = fold16(z1, z3);
    u0 += dpp<ROR8>(u0); u1 += dpp<ROR8>(u1);
    if ((lane & 8) == 0 && c < 5)
      *reinterpret_cast<f32x2*>(lds + L_YR + wave * 40 + 8 * c + 4 * (lane >> 5) + 2 * ((lane >> 4) & 1)) = f32x2{u0, u1};
  }
  __syncthreads();
  if (tid < 20) {
    float y0 = 0.f, y1 = 0.f;
#pragma unroll
    for (int w = 0; w < NWAVE; ++w) { const f32x2 v = *reinterpret_cast<const f32x2*>(lds + L_YR + w * 40 + 2 * tid); y0 += v.x; y1 += v.y; }
    __builtin_amdgcn_raw_buffer_store_b128(pack2(y0, y1, t8, thi), rw, tid * 16, WS_Y + ((b * 8 + team) * 320 + cb * 20) * 16, AUX_SC1);
  }
  STAMP(5);
  if (wave < 5) {
    u32x4 gy[8];
    for (unsigned spins = 0;; ++spins) {
      unsigned bad = 0;
      asm volatile("" ::: "memory");
#pragma unroll
      for (int s = 0; s < 8; ++s) gy[s] = LD128(rw, tid * 16, WS_Y + (b * 8 + s) * 320 * 16, AUX_SC1);
#pragma unroll
      for (int s = 0; s < 8; ++s) bad |= tag_bad(gy[s], t8, thi);
      if (__all(bad == 0u) || spins > SPIN_CAP) break;
      __builtin_amdgcn_s_sleep(2);
    }
    float a0 = 0.f, a1 = 0.f;
#pragma unroll
    for (int s = 0; s < 8; ++s) { a0 += g0(gy[s]); a1 += g1(gy[s]); }
    *reinterpret_cast<f32x2*>(lds + L_H + 2 * tid) = f32x2{bflo(xres) + a0, bfhi(xres) + a1};
  }
  __syncthreads();
  STAMP(6);
  {
    float hv[10];
#pragma unroll
    for (int i = 0; i < 5; ++i) { const f32x2 v = *reinterpret_cast<const f32x2*>(lds + L_H + 2 * lane + 128 * i); hv[2 * i] = v.x; hv[2 * i + 1] = v.y; }
    float s = 0.f;
#pragma unroll
    for (int i = 0; i < 10; ++i) s += hv[i];
    const float mean1 = wave_sum(s) * (1.0f / D);
    float v = 0.f;
#pragma unroll
    for (int i = 0; i < 10; ++i) { hv[i] -= mean1; v += hv[i] * hv[i]; }
    const float rs1 = __builtin_amdgcn_rsqf(wave_sum(v) * (1.0f / D) + 1e-5f);
    s = 0.f;
#pragma unroll
    for (int i = 0; i < 10; ++i) { hv[i] *= rs1; s += hv[i]; }
    const float mean2 = wave_sum(s) * (1.0f / D);
    v = 0.f;
#pragma unroll
    for (int i = 0; i < 10; ++i) { const float a = hv[i] - mean2; v += a * a; }
    const float rs2 = __builtin_amdgcn_rsqf(wave_sum(v) * (1.0f / D) + 1e-5f);
    const int d0 = 5 * (team * 16 + cb);
    float ya[5];
#pragma unroll
    for (int e = 0; e < 5; ++e) {
      const float t = ((lds[L_H + d0 + e] - mean1) * rs1 - mean2) * rs2;
      float a = 0.f;
#pragma unroll
      for (int k = 0; k < 3; ++k) {
        const float u = fmaf(t, bflo(mw1[k]), bflo(mb1[k]));
        const float z = u * fmaf(u * u, -2.0f * 0.7978845608028654f * 0.044715f * 1.4426950408889634f, -2.0f * 0.7978845608028654f * 1.4426950408889634f);
        const float g = u * __builtin_amdgcn_rcpf(1.0f + __builtin_amdgcn_exp2f(z));
        a = fmaf(g, bflo(mw2[k]), a);
      }
      ya[e] = wave_sum(a);
    }
    if (lane < 5) {
      float yv = ya[0];
#pragma unroll
      for (int e = 1; e < 5; ++e) yv = lane == e ? ya[e] : yv;
      lds[L_MR + wave * 8 + lane] = yv;
    }
    __syncthreads();
    if (tid < 5) {
      float yv = 0.f;
#pragma unroll
      for (int w = 0; w < NWAVE; ++w) yv += lds[L_MR + w * 8 + tid];
      const unsigned o = pack_bf16(lds[L_H + d0 + tid] + yv + bflo(mb2), 0.f);
      __builtin_amdgcn_raw_buffer_store_b16((unsigned short)o, __builtin_amdgcn_make_buffer_rsrc((void*)out, 0, 2 * D * 2 + 4, RSRC3),
                                            tid * 2, (b * D + d0) * 2, 0);
    }
    if (tid == 0 && team == 0 && cb == 0)
      __builtin_amdgcn_raw_buffer_store_b64(u32x2{(unsigned)tag, (unsigned)(tag >> 32)}, rw, 0, WS_EPOCH + b * 256, AUX_SC1);
  }
  STAMP(7);
}

}

extern "C" void kernel_launch(void* const* d_in, const int* in_sizes, int n_in, void* d_out, int out_size, void* d_ws,
                              size_t ws_size, hipStream_t stream) {
  (void)in_sizes; (void)n_in; (void)out_size; (void)ws_size;
  fused_kernel<<<dim3(NWG), dim3(NTHR), 0, stream>>>(
      (const unsigned*)d_in[0], (const char*)d_in[1], (const char*)d_in[2], (const char*)d_in[3], (const char*)d_in[4],
      (const unsigned short*)d_in[6], (const unsigned short*)d_in[7], (const unsigned short*)d_in[8],
      (const unsigned short*)d_in[9], (__hip_bfloat16*)d_out, (char*)d_ws);
}
```

```cpp
#include <hip/hip_runtime.h>
#include <hip/hip_bf16.h>
#include <stdint.h>

namespace {

typedef unsigned u32x4 __attribute__((ext_vector_type(4)));
typedef unsigned u32x2 __attribute__((ext_vector_type(2)));
typedef float f32x2 __attribute__((ext_vector_type(2)));
typedef __bf16 bf16x2 __attribute__((ext_vector_type(2)));

constexpr int D = 640, H = 32, SK = 8192, DH = 192, FF = 1536;
constexpr int NWG = 256, NTHR = 512, NWAVE = 8;
constexpr unsigned TILE_BYTES = 8 * DH * 2;
constexpr unsigned HEAD_BYTES = SK * DH * 2;
#define NOQ 0
#define NOWO 0
#define RA 8
#define PSLEEP 0
#define CATCH 3
#define RPRE 5
constexpr int GS = 4;
constexpr int NSW = 4, NH = NWAVE - NSW;
constexpr int STEPS = SK / 8 / (GS * NSW);
constexpr unsigned STRIDE = GS * NSW * TILE_BYTES;
constexpr int AUX_NT = 2, AUX_SC1 = 16;
constexpr unsigned RSRC3 = 0x00020000u;
constexpr unsigned OOB = 0x80000000u;

constexpr unsigned WS_EPOCH = 0;
constexpr unsigned WS_RDY = 4096;
constexpr unsigned WS_P = 8192;
constexpr unsigned WS_Y = WS_P + NWG * 1024;
constexpr unsigned WS_QP = WS_Y + 2 * 8 * 320 * 16;
constexpr unsigned WS_END = WS_QP + 32 * 2 * 8 * 96 * 16;

constexpr int L_RED = 0;
constexpr int L_ML = L_RED + NSW * DH;
constexpr int L_Q = L_ML + 16;
constexpr int L_TAG = L_Q + 96;
constexpr int L_O = L_TAG + 4;
constexpr int L_YR = L_O + 384;
constexpr int L_H = L_YR + 320;
constexpr int L_ST = L_H + 320;
constexpr int L_MR = L_ST + 16;
constexpr int L_WO = L_MR + 64;
constexpr int L_TOTAL = 24 * 1024;
static_assert(L_WO + 24 * 2 * 64 * 4 <= L_TOTAL, "lds");
static_assert(L_WO % 4 == 0 && L_Q % 4 == 0 && L_ST % 4 == 0 && L_MR % 4 == 0, "alignment");

__device__ __forceinline__ float dot2(unsigned a, unsigned b, float c) {
  return __builtin_amdgcn_fdot2_f32_bf16(__builtin_bit_cast(bf16x2, a), __builtin_bit_cast(bf16x2, b), c, false);
}
template <int CTRL> __device__ __forceinline__ float dpp(float x) {
  return __builtin_bit_cast(float, __builtin_amdgcn_mov_dpp(__builtin_bit_cast(int, x), CTRL, 0xf, 0xf, true));
}
constexpr int XOR1 = 0xB1, XOR2 = 0x4E, HMIR = 0x141, ROR8 = 0x128;
constexpr unsigned ONES = 0x3f803f80u;
__device__ __forceinline__ float bflo(unsigned u) { return __builtin_bit_cast(float, u << 16); }
__device__ __forceinline__ float bfhi(unsigned u) { return __builtin_bit_cast(float, u & 0xffff0000u); }
__device__ __forceinline__ f32x2 bf2(unsigned u) { return f32x2{bflo(u), bfhi(u)}; }
__device__ __forceinline__ unsigned pack_bf16(float lo, float hi) {
  bf16x2 v; v[0] = (__bf16)lo; v[1] = (__bf16)hi;
  return __builtin_bit_cast(unsigned, v);
}
__device__ __forceinline__ float rbf(float x) {
  typedef float f32x2_ __attribute__((ext_vector_type(2)));
  return __builtin_bit_cast(float, __builtin_convertvector(f32x2_{0.f, x}, bf16x2));
}
__device__ __forceinline__ float fold32(float a, float b) {
  auto r = __builtin_amdgcn_permlane32_swap(__float_as_uint(a), __float_as_uint(b), false, false);
  return __uint_as_float(r[0]) + __uint_as_float(r[1]);
}
__device__ __forceinline__ float fold16(float a, float b) {
  auto r = __builtin_amdgcn_permlane16_swap(__float_as_uint(a), __float_as_uint(b), false, false);
  return __uint_as_float(r[0]) + __uint_as_float(r[1]);
}
__device__ __forceinline__ float fold8(float a, float b, bool bit3) {
  const float keep = bit3 ? b : a, send = bit3 ? a : b;
  return keep + dpp<ROR8>(send);
}
__device__ __forceinline__ float max3216(float x) {
  auto s = __builtin_amdgcn_permlane16_swap(__float_as_uint(x), __float_as_uint(x), false, false);
  x = fmaxf(__uint_as_float(s[0]), __uint_as_float(s[1]));
  auto t = __builtin_amdgcn_permlane32_swap(__float_as_uint(x), __float_as_uint(x), false, false);
  return fmaxf(__uint_as_float(t[0]), __uint_as_float(t[1]));
}
__device__ __forceinline__ float row_sum16(float x) {
  x += dpp<XOR1>(x); x += dpp<XOR2>(x); x += dpp<HMIR>(x); x += dpp<ROR8>(x);
  return x;
}
__device__ __forceinline__ unsigned long long mix64(unsigned long long z) {
  z += 0x9E3779B97F4A7C15ull;
  z = (z ^ (z >> 30)) * 0xBF58476D1CE4E5B9ull;
  z = (z ^ (z >> 27)) * 0x94D049BB133111EBull;
  return z ^ (z >> 31);
}
__device__ __forceinline__ u32x4 pack2(float a, float b, unsigned t8, unsigned thi) {
  u32x4 r; r.x = (__float_as_uint(a) >> 8) | t8; r.y = thi; r.z = (__float_as_uint(b) >> 8) | t8; r.w = thi; return r;
}
__device__ __forceinline__ unsigned tag_bad(u32x4 g, unsigned t8, unsigned thi) {
  return (((g.x ^ t8) | (g.z ^ t8)) >> 24) | (g.y ^ thi) | (g.w ^ thi);
}
__device__ __forceinline__ float g0(u32x4 g) { return __uint_as_float(g.x << 8); }
__device__ __forceinline__ float g1(u32x4 g) { return __uint_as_float(g.z << 8); }
__device__ __forceinline__ f32x2 g01(u32x4 g) { return f32x2{g0(g), g1(g)}; }
__device__ __forceinline__ unsigned vbit(float x) { return __float_as_uint(x) | 1u; }
__device__ __forceinline__ f32x2 asf2(unsigned a, unsigned b) { return f32x2{__uint_as_float(a), __uint_as_float(b)}; }
__device__ __forceinline__ float ln_den_inv(float var) {
  return __builtin_amdgcn_rcpf(rbf(__builtin_amdgcn_sqrtf(rbf(rbf(var) + 1e-5f))));
}
__device__ __forceinline__ float rdlane(float x, int l) { return __uint_as_float(__builtin_amdgcn_readlane(__float_as_uint(x), l)); }
__device__ __forceinline__ f32x2 splat(float x) { return f32x2{x, x}; }
__device__ __forceinline__ f32x2 fma2(f32x2 a, f32x2 b, f32x2 c) { return __builtin_elementwise_fma(a, b, c); }

#define SB() __builtin_amdgcn_sched_barrier(0)
#define MEMBAR() asm volatile("" ::: "memory")
#define LD128(rs, vo, so, aux) __builtin_amdgcn_raw_buffer_load_b128((rs), (vo), (so), (aux))
#define ST128(v, rs, vo, so) __builtin_amdgcn_raw_buffer_store_b128((v), (rs), (vo), (so), AUX_SC1)
#define LD64(rs, vo, so, aux) __builtin_amdgcn_raw_buffer_load_b64((rs), (vo), (so), (aux))
#define ST64(v, rs, vo, so) __builtin_amdgcn_raw_buffer_store_b64((v), (rs), (vo), (so), AUX_SC1)
#define WAIT_VM3() __builtin_amdgcn_s_waitcnt(0x0F73)
#define STAMP(i) do { } while (0)

constexpr unsigned SPIN_CAP = 1u << 17;

__global__ void __launch_bounds__(NTHR, 2)
fused_kernel(const char* x, const char* kc, const char* vc, const char* wq, const char* wo, const char* w1,
             const char* b1, const char* w2, const char* b2, char* out, char* ws) {
  __shared__ __attribute__((aligned(16))) float lds[L_TOTAL];
  unsigned* const ldu = reinterpret_cast<unsigned*>(lds);
  const int tid = threadIdx.x, lane = tid & 63, wave = __builtin_amdgcn_readfirstlane(tid >> 6);
  const int bid = blockIdx.x, team = bid & 7, jj = bid >> 3, hl = jj >> 3, b = (jj >> 2) & 1, sp = jj & 3;
  const int h = team * 4 + hl, cb = hl * 4 + sp, pq = jj & 7;
  const int r = lane >> 3, c = lane & 7;
  const bool bit3 = (lane & 8) != 0;
  const int fidx = 8 * c + 4 * (lane >> 5) + 2 * ((lane >> 4) & 1) + (bit3 ? 1 : 0);
  STAMP(0);
  asm volatile("" : "+s"(x), "+s"(kc), "+s"(vc), "+s"(wq), "+s"(wo), "+s"(w1), "+s"(b1), "+s"(w2), "+s"(b2), "+s"(out), "+s"(ws));
  const __amdgpu_buffer_rsrc_t rw = __builtin_amdgcn_make_buffer_rsrc((void*)ws, 0, WS_END, RSRC3);
  const __amdgpu_buffer_rsrc_t rx = __builtin_amdgcn_make_buffer_rsrc((void*)x, 0, 2 * D * 2, RSRC3);
  constexpr float SCALE = 0.07216878364870322f, LOG2E = 1.4426950408889634f;
  unsigned vzero;
  asm volatile("v_mov_b32 %0, 0" : "=v"(vzero));

  f32x2 fw1, fb1, fw2; float gw1, gb1, gw2, fb2;
  unsigned xres;
#define LOAD_SMALL() \
    unsigned short a1[3], a2[3], a3[3], a4; { \
    const __amdgpu_buffer_rsrc_t r1 = __builtin_amdgcn_make_buffer_rsrc((void*)w1, 0, FF * 2 + 4, RSRC3); \
    const __amdgpu_buffer_rsrc_t r2 = __builtin_amdgcn_make_buffer_rsrc((void*)b1, 0, FF * 2 + 4, RSRC3); \
    const __amdgpu_buffer_rsrc_t r3 = __builtin_amdgcn_make_buffer_rsrc((void*)w2, 0, FF * 2 + 4, RSRC3); \
    _Pragma("unroll") for (int k = 0; k < 3; ++k) { \
      a1[k] = __builtin_amdgcn_raw_buffer_load_b16(r1, tid * 2 + 1024 * k, 0, 0); \
      a2[k] = __builtin_amdgcn_raw_buffer_load_b16(r2, tid * 2 + 1024 * k, 0, 0); \
      a3[k] = __builtin_amdgcn_raw_buffer_load_b16(r3, tid * 2 + 1024 * k, 0, 0); \
    } \
    a4 = __builtin_amdgcn_raw_buffer_load_b16(__builtin_amdgcn_make_buffer_rsrc((void*)b2, 0, 4, RSRC3), vzero, 0, 0); \
    xres = __builtin_amdgcn_raw_buffer_load_b32(rx, tid >= 192 ? (tid - 192) * 4 : OOB, b * D * 2, 0); \
    MEMBAR(); }
#define CONV_SMALL() { \
    fw1 = f32x2{bflo(a1[0]), bflo(a1[1])}; fb1 = f32x2{bflo(a2[0]), bflo(a2[1])}; fw2 = f32x2{bflo(a3[0]), bflo(a3[1])}; \
    gw1 = bflo(a1[2]); gb1 = bflo(a2[2]); gw2 = bflo(a3[2]); fb2 = bflo(a4); \
    asm volatile("" : "+v"(fw1), "+v"(fb1), "+v"(fw2), "+v"(gw1), "+v"(gb1), "+v"(gw2), "+v"(fb2), "+v"(xres) :: "memory"); }

  if (wave >= NH) {
    const int sw = wave - NH;
    const size_t kvbase = (size_t)(b * H + h) * HEAD_BYTES;
    const __amdgpu_buffer_rsrc_t rk = __builtin_amdgcn_make_buffer_rsrc((void*)(kc + kvbase), 0, HEAD_BYTES, RSRC3);
    const __amdgpu_buffer_rsrc_t rv = __builtin_amdgcn_make_buffer_rsrc((void*)(vc + kvbase), 0, HEAD_BYTES, RSRC3);
    const unsigned kvo = r * (DH * 2) + c * 16;
    u32x4 kbuf[RPRE][3], vbuf[RPRE][3];
    unsigned so = (sp * NSW + sw) * TILE_BYTES + (h & 1) * RA * STRIDE;
    static_assert(RA + RPRE + 2 < STEPS, "no wrap before the main loop");
#pragma unroll
    for (int j = 0; j < RPRE; ++j) {
      if (j) { WAIT_VM3(); SB(); }
#pragma unroll
      for (int t = 0; t < 3; ++t) { kbuf[j][t] = LD128(rk, kvo + t * 128, so, AUX_NT); SB(); }
      if (j) { WAIT_VM3(); SB(); }
#pragma unroll
      for (int t = 0; t < 3; ++t) { vbuf[j][t] = LD128(rv, kvo + t * 128, so, AUX_NT); SB(); }
      so += STRIDE;
    }
    LOAD_SMALL();
    STAMP(8);
    __syncthreads();
    u32x4 q[3];
#pragma unroll
    for (int t = 0; t < 3; ++t) q[t] = *reinterpret_cast<const u32x4*>(ldu + L_Q + (8 * t + c) * 4);
    STAMP(1);
    float m = -INFINITY, l = 0.f;
    f32x2 acc[12];
#pragma unroll
    for (int k = 0; k < 12; ++k) acc[k] = f32x2{0.f, 0.f};

#define PIN12(A) asm volatile("" : "+v"(A[0]), "+v"(A[1]), "+v"(A[2]), "+v"(A[3]), "+v"(A[4]), "+v"(A[5]), "+v"(A[6]), "+v"(A[7]), \
    "+v"(A[8]), "+v"(A[9]), "+v"(A[10]), "+v"(A[11]) :: "memory")
#define STEP(KB, VB, REFILL) { \
    float s0_ = 0.f, s1_ = 0.f, s2_ = 0.f; \
    _Pragma("unroll") for (int j_ = 0; j_ < 4; ++j_) { s0_ = dot2(KB[0][j_], q[0][j_], s0_); s1_ = dot2(KB[1][j_], q[1][j_], s1_); s2_ = dot2(KB[2][j_], q[2][j_], s2_); } \
    asm volatile("" : "+v"(s0_), "+v"(s1_), "+v"(s2_) :: "memory"); \
    SB(); \
    if (REFILL) { _Pragma("unroll") for (int u_ = 0; u_ < 3; ++u_) KB[u_] = LD128(rk, kvo + u_ * 128, so, AUX_NT); } \
    SB(); \
    float s_ = s0_ + s1_ + s2_; \
    s_ += dpp<XOR1>(s_); s_ += dpp<XOR2>(s_); s_ += dpp<HMIR>(s_); \
    s_ = rbf(rbf(s_) * SCALE) * LOG2E; \
    if (__any(s_ > m)) { \
      const float mn_ = fmaxf(m, s_); \
      const float al_ = __builtin_amdgcn_exp2f(m - mn_); \
      m = mn_; l *= al_; \
      _Pragma("unroll") for (int k_ = 0; k_ < 12; ++k_) acc[k_] *= splat(al_); \
    } \
    float p_ = __builtin_amdgcn_exp2f(s_ - m); \
    asm volatile("" : "+v"(p_)); \
    SB(); \
    f32x2 vf_[12]; \
    _Pragma("unroll") for (int t_ = 0; t_ < 3; ++t_) \
      _Pragma("unroll") for (int j_ = 0; j_ < 4; ++j_) vf_[t_ * 4 + j_] = bf2(VB[t_][j_]); \
    PIN12(vf_); \
    SB(); \
    if (REFILL) { _Pragma("unroll") for (int u_ = 0; u_ < 3; ++u_) VB[u_] = LD128(rv, kvo + u_ * 128, so, AUX_NT); } \
    SB(); \
    l += p_; \
    _Pragma("unroll") for (int k_ = 0; k_ < 12; ++k_) acc[k_] = fma2(splat(p_), vf_[k_], acc[k_]); \
    PIN12(acc); \
    SB(); }

#define STEP2(KB, VB, KN, VN) { \
    __builtin_amdgcn_s_waitcnt(0x0F73); \
    SB(); \
    _Pragma("unroll") for (int u_ = 0; u_ < 3; ++u_) KN[u_] = LD128(rk, kvo + u_ * 128, so, AUX_NT); \
    MEMBAR(); \
    SB(); \
    float s0_ = 0.f, s1_ = 0.f, s2_ = 0.f; \
    _Pragma("unroll") for (int j_ = 0; j_ < 4; ++j_) { s0_ = dot2(KB[0][j_], q[0][j_], s0_); s1_ = dot2(KB[1][j_], q[1][j_], s1_); s2_ = dot2(KB[2][j_], q[2][j_], s2_); } \
    float s_ = s0_ + s1_ + s2_; \
    s_ += dpp<XOR1>(s_); s_ += dpp<XOR2>(s_); s_ += dpp<HMIR>(s_); \
    s_ = rbf(rbf(s_) * SCALE) * LOG2E; \
    if (__any(s_ > m)) { \
      const float mn_ = fmaxf(m, s_); \
      const float al_ = __builtin_amdgcn_exp2f(m - mn_); \
      m = mn_; l *= al_; \
      _Pragma("unroll") for (int k_ = 0; k_ < 12; ++k_) acc[k_] *= splat(al_); \
    } \
    float p_ = __builtin_amdgcn_exp2f(s_ - m); \
    asm volatile("" : "+v"(p_) :: "memory"); \
    SB(); \
    __builtin_amdgcn_s_waitcnt(0x0F73); \
    SB(); \
    _Pragma("unroll") for (int u_ = 0; u_ < 3; ++u_) VN[u_] = LD128(rv, kvo + u_ * 128, so, AUX_NT); \
    MEMBAR(); \
    SB(); \
    l += p_; \
    _Pragma("unroll") for (int t_ = 0; t_ < 3; ++t_) \
      _Pragma("unroll") for (int j_ = 0; j_ < 4; ++j_) acc[t_ * 4 + j_] = fma2(splat(p_), bf2(VB[t_][j_]), acc[t_ * 4 + j_]); \
    PIN12(acc); \
    SB(); }

    static_assert(RPRE % 2 == 1 && CATCH >= 1 && CATCH < RPRE, "parity of the main loop");
    STEP(kbuf[0], vbuf[0], true);
#pragma unroll
    for (int jb = 1; jb <= CATCH; ++jb) STEP(kbuf[jb], vbuf[jb], false);
    so += STRIDE;
    WAIT_VM3(); SB();
#pragma unroll
    for (int t = 0; t < 3; ++t) kbuf[1][t] = LD128(rk, kvo + t * 128, so, AUX_NT);
    SB(); WAIT_VM3(); SB();
#pragma unroll
    for (int t = 0; t < 3; ++t) vbuf[1][t] = LD128(rv, kvo + t * 128, so, AUX_NT);
    MEMBAR(); SB();
#pragma unroll
    for (int jb = CATCH + 1; jb < RPRE; ++jb) STEP(kbuf[jb], vbuf[jb], false);
    STEP(kbuf[0], vbuf[0], false);
    CONV_SMALL();
#pragma nounroll
    for (int i = RPRE + 1; i < STEPS; i += 2) {
      so += STRIDE;
      if (so >= HEAD_BYTES) so -= HEAD_BYTES;
      STEP2(kbuf[1], vbuf[1], kbuf[0], vbuf[0]);
      so += STRIDE;
      if (so >= HEAD_BYTES) so -= HEAD_BYTES;
      if (i == STEPS - 2) so = OOB;
      STEP2(kbuf[0], vbuf[0], kbuf[1], vbuf[1]);
    }
    STAMP(2);
    {
      float mw = fmaxf(m, dpp<ROR8>(m));
      mw = max3216(mw);
      const float f = __builtin_amdgcn_exp2f(m - mw);
      float lw = l * f;
      lw += dpp<ROR8>(lw); lw = fold16(lw, lw); lw = fold32(lw, lw);
#pragma unroll
      for (int k = 0; k < 12; ++k) acc[k] *= splat(f);
      float* const red = lds + L_RED + sw * DH + fidx;
#pragma unroll
      for (int t = 0; t < 3; ++t) {
        const float z0 = fold32(acc[t * 4 + 0].x, acc[t * 4 + 2].x), z1 = fold32(acc[t * 4 + 0].y, acc[t * 4 + 2].y);
        const float z2 = fold32(acc[t * 4 + 1].x, acc[t * 4 + 3].x), z3 = fold32(acc[t * 4 + 1].y, acc[t * 4 + 3].y);
        red[64 * t] = fold8(fold16(z0, z2), fold16(z1, z3), bit3);
      }
      if (lane == 0) *reinterpret_cast<f32x2*>(lds + L_ML + 2 * sw) = f32x2{mw, lw};
    }
    __builtin_amdgcn_s_waitcnt(0x0F70);
  } else {
    if (wave < 3 && !NOQ) {
      const __amdgpu_buffer_rsrc_t rq = __builtin_amdgcn_make_buffer_rsrc((void*)wq, 0, D * H * DH * 2, RSRC3);
      unsigned xs[2][5], xp[2];
#pragma unroll
      for (int bb = 0; bb < 2; ++bb) {
#pragma unroll
        for (int i = 0; i < 5; ++i) xs[bb][i] = __builtin_amdgcn_raw_buffer_load_b32(rx, lane * 4 + i * 256 + bb * D * 2, 0, 0);
        xp[bb] = __builtin_amdgcn_raw_buffer_load_b32(rx, (c < 5 ? (8 * c + r) * 4 : 0) + bb * D * 2, pq * 160, 0);
      }
      const u32x2 ep = __builtin_amdgcn_raw_buffer_load_b64(rw, vzero, WS_EPOCH, AUX_SC1);
      SB();
      u32x4 wqr[5][2];
      {
        const unsigned vo = (unsigned)(2 * r) * (H * DH * 2) + c * 16;
        const unsigned qso = (unsigned)(80 * pq) * (H * DH * 2) + h * (DH * 2) + wave * 128;
#pragma unroll
        for (int i = 0; i < 5; ++i)
#pragma unroll
          for (int e = 0; e < 2; ++e) { wqr[i][e] = LD128(rq, vo, qso + (16 * i + e) * (H * DH * 2), AUX_NT); SB(); }
      }
      float s0 = 0.f, s1 = 0.f, q0 = 0.f, q1 = 0.f;
#pragma unroll
      for (int i = 0; i < 5; ++i) {
        s0 = dot2(xs[0][i], ONES, s0); q0 = dot2(xs[0][i], xs[0][i], q0);
        s1 = dot2(xs[1][i], ONES, s1); q1 = dot2(xs[1][i], xs[1][i], q1);
      }
      const float st = row_sum16(fold16(fold32(s0, s1), fold32(q0, q1)));
      auto sw2 = __builtin_amdgcn_permlane16_swap(__float_as_uint(st), __float_as_uint(st), false, false);
      const float mean = __uint_as_float(sw2[0]) * (1.0f / D);
      const float var = fmaxf(fmaf(-mean, mean, __uint_as_float(sw2[1]) * (1.0f / D)), 0.f);
      const float inv = ln_den_inv(var), mb = rbf(mean);
      const float m0 = rdlane(mb, 0), m1 = rdlane(mb, 32), i0 = rdlane(inv, 0), i1 = rdlane(inv, 32);
      const unsigned xn0 = pack_bf16(rbf(bflo(xp[0]) - m0) * i0, rbf(bfhi(xp[0]) - m0) * i0);
      const unsigned xn1 = pack_bf16(rbf(bflo(xp[1]) - m1) * i1, rbf(bfhi(xp[1]) - m1) * i1);
      const unsigned long long tg = mix64(((unsigned long long)(unsigned)__builtin_amdgcn_readfirstlane(ep.y) << 32) | (unsigned)__builtin_amdgcn_readfirstlane(ep.x));
      const unsigned thi = (unsigned)(tg >> 32), t8 = (unsigned)tg & 0xff000000u;
      float a0[8], a1q[8];
#pragma unroll
      for (int k = 0; k < 8; ++k) { a0[k] = 0.f; a1q[k] = 0.f; }
#define QGROUP(I) { \
        unsigned b0_ = __builtin_amdgcn_update_dpp(0u, xn0, 0x150 + I, 0xf, 0x3, false); b0_ = __builtin_amdgcn_update_dpp(b0_, xn0, 0x158 + I, 0xf, 0xc, false); \
        unsigned b1_ = __builtin_amdgcn_update_dpp(0u, xn1, 0x150 + I, 0xf, 0x3, false); b1_ = __builtin_amdgcn_update_dpp(b1_, xn1, 0x158 + I, 0xf, 0xc, false); \
        _Pragma("unroll") for (int j = 0; j < 4; ++j) { \
          const unsigned plo = __builtin_amdgcn_perm(wqr[I][1][j], wqr[I][0][j], 0x05040100u), phi = __builtin_amdgcn_perm(wqr[I][1][j], wqr[I][0][j], 0x07060302u); \
          a0[2 * j] = dot2(plo, b0_, a0[2 * j]); a0[2 * j + 1] = dot2(phi, b0_, a0[2 * j + 1]); \
          a1q[2 * j] = dot2(plo, b1_, a1q[2 * j]); a1q[2 * j + 1] = dot2(phi, b1_, a1q[2 * j + 1]); } }
      QGROUP(0) QGROUP(1) QGROUP(2) QGROUP(3) QGROUP(4)
      float z[8];
#pragma unroll
      for (int k = 0; k < 8; ++k) z[k] = fold32(a0[k], a1q[k]);
      float y[4];
#pragma unroll
      for (int k = 0; k < 4; ++k) y[k] = fold16(z[k], z[k + 4]);
      const float u0 = fold8(y[0], y[2], bit3), u1 = fold8(y[1], y[3], bit3);
      const unsigned pidx = 32 * wave + 4 * c + 2 * ((lane >> 4) & 1) + (bit3 ? 1 : 0);
      ST128(pack2(u0, u1, t8, thi), rw, ((lane >> 5) * 8 * 96 + pidx) * 16, WS_QP + (h * 16 + pq) * (96 * 16));
      STAMP(8);
      asm volatile("s_waitcnt vmcnt(0)" ::: "memory");
      STAMP(10);
      {
        const unsigned qo = ((lane >> 5) * 96 + 32 * wave + (lane & 31)) * 16;
        const unsigned qb = WS_QP + (h * 16 + b * 8) * (96 * 16);
        u32x4 gq[4];
        for (unsigned spins = 0;; ++spins) {
          MEMBAR();
#pragma unroll
          for (int k = 0; k < 4; ++k) gq[k] = LD128(rw, qo, qb + k * (2 * 96 * 16), AUX_SC1);
          unsigned bad = 0;
#pragma unroll
          for (int k = 0; k < 4; ++k) bad |= tag_bad(gq[k], t8, thi);
          if (__all(bad == 0u) || spins > SPIN_CAP) break;
        }
        const f32x2 qq = (g01(gq[0]) + g01(gq[1])) + (g01(gq[2]) + g01(gq[3]));
        const float qa = fold32(qq.x, qq.x), qc = fold32(qq.y, qq.y);
        if (lane < 32) ldu[L_Q + 32 * wave + lane] = pack_bf16(qa, qc);
      }
      if (tid == 0) *reinterpret_cast<u32x2*>(ldu + L_TAG) = u32x2{(unsigned)tg, thi};
    }
    const unsigned yoff = ((lane >= 20 ? 8 * 320 : 0) + (lane >= 20 ? lane - 20 : lane)) * 16;
    const unsigned ybo = WS_Y + (team * 320 + cb * 20) * 16 + b * 8;
    if (wave == 3) {
      const u32x2 ep = LD64(rw, vzero, WS_EPOCH, AUX_SC1);
      ST128((u32x4{0u, 0u, 0u, 0u}), rw, lane * 16, WS_P + bid * 1024);
      ST64((u32x2{0u, 0u}), rw, lane < 40 ? yoff : OOB, ybo);
      asm volatile("s_waitcnt vmcnt(0)" ::: "memory");
      const unsigned long long tg = mix64(((unsigned long long)(unsigned)__builtin_amdgcn_readfirstlane(ep.y) << 32) | (unsigned)__builtin_amdgcn_readfirstlane(ep.x));
      ST64((u32x2{(unsigned)tg, (unsigned)(tg >> 32)}), rw, lane == 0 ? 0u : OOB, WS_RDY + bid * 8);
    }
    if (NOQ && wave == 0) {
      const u32x2 ep = __builtin_amdgcn_raw_buffer_load_b64(rw, vzero, WS_EPOCH, AUX_SC1);
      const unsigned long long tg = mix64(((unsigned long long)(unsigned)__builtin_amdgcn_readfirstlane(ep.y) << 32) | (unsigned)__builtin_amdgcn_readfirstlane(ep.x));
      if (tid == 0) *reinterpret_cast<u32x2*>(ldu + L_TAG) = u32x2{(unsigned)tg, (unsigned)(tg >> 32)};
      ldu[L_Q + lane] = 0x3c003c00; ldu[L_Q + 32 + lane] = 0x3c003c00;
    }
    STAMP(9);
    __syncthreads();
    STAMP(1);
    LOAD_SMALL();
    const __amdgpu_buffer_rsrc_t ro = __builtin_amdgcn_make_buffer_rsrc((void*)wo, 0, D * H * DH * 2, RSRC3);
    const unsigned vo = c < 5 ? (unsigned)(2 * r) * (D * 2) + c * 16 : OOB;
#pragma nounroll
    for (int g = wave; g < (NOWO ? 0 : 24); g += NH) {
      const unsigned wso = (unsigned)(768 * team + 384 * b + 16 * g) * (D * 2) + cb * 80;
      const u32x4 e0 = LD128(ro, vo, wso, AUX_NT);
      const u32x4 e1 = LD128(ro, vo + D * 2, wso, AUX_NT);
      u32x4 p0, p1;
      p0.x = __builtin_amdgcn_perm(e1.x, e0.x, 0x05040100u); p0.y = __builtin_amdgcn_perm(e1.x, e0.x, 0x07060302u);
      p0.z = __builtin_amdgcn_perm(e1.y, e0.y, 0x05040100u); p0.w = __builtin_amdgcn_perm(e1.y, e0.y, 0x07060302u);
      p1.x = __builtin_amdgcn_perm(e1.z, e0.z, 0x05040100u); p1.y = __builtin_amdgcn_perm(e1.z, e0.z, 0x07060302u);
      p1.z = __builtin_amdgcn_perm(e1.w, e0.w, 0x05040100u); p1.w = __builtin_amdgcn_perm(e1.w, e0.w, 0x07060302u);
      *reinterpret_cast<u32x4*>(ldu + L_WO + ((g * 2 + 0) * 64 + lane) * 4) = p0;
      *reinterpret_cast<u32x4*>(ldu + L_WO + ((g * 2 + 1) * 64 + lane) * 4) = p1;
    }
    CONV_SMALL();
    if (wave == 0) {
      const u32x2 tg = *reinterpret_cast<const u32x2*>(ldu + L_TAG);
      for (unsigned spins = 0; spins < SPIN_CAP; ++spins) {
        MEMBAR();
        const u32x4 r0 = LD128(rw, lane * 16, WS_RDY, AUX_SC1), r1 = LD128(rw, lane * 16 + 1024, WS_RDY, AUX_SC1);
        const unsigned bad = (r0.x ^ tg.x) | (r0.y ^ tg.y) | (r0.z ^ tg.x) | (r0.w ^ tg.y) | (r1.x ^ tg.x) | (r1.y ^ tg.y) | (r1.z ^ tg.x) | (r1.w ^ tg.y);
        if (__all(bad == 0u)) break;
        __builtin_amdgcn_s_sleep(16);
      }
    }
  }
  const u32x2 tg2 = *reinterpret_cast<const u32x2*>(ldu + L_TAG);
  const unsigned tlo = __builtin_amdgcn_readfirstlane(tg2.x), thi = __builtin_amdgcn_readfirstlane(tg2.y);

  __syncthreads();
  if (tid < 97) {
    const int d2 = tid < 96 ? 2 * tid : 0;
    f32x2 ml[NSW], v[NSW];
#pragma unroll
    for (int w = 0; w < NSW; ++w) {
      ml[w] = *reinterpret_cast<const f32x2*>(lds + L_ML + 2 * w);
      v[w] = *reinterpret_cast<const f32x2*>(lds + L_RED + w * DH + d2);
    }
    const float M = __uint_as_float(vbit(fmaxf(fmaxf(ml[0].x, ml[1].x), fmaxf(ml[2].x, ml[3].x))));
    float Ls = 0.f; f32x2 o = {0.f, 0.f};
#pragma unroll
    for (int w = 0; w < NSW; ++w) {
      const float f = __builtin_amdgcn_exp2f(ml[w].x - M);
      Ls = fmaf(f, ml[w].y, Ls); o = fma2(splat(f), v[w], o);
    }
    if (tid == 96) o = f32x2{M, Ls};
    ST64((u32x2{vbit(o.x), vbit(o.y)}), rw, tid * 8, WS_P + bid * 1024);
  }
  STAMP(3);
  u32x4 wop[6][2];
#pragma unroll
  for (int i = 0; i < 6; ++i) {
    wop[i][0] = *reinterpret_cast<const u32x4*>(ldu + L_WO + (((6 * (wave & 3) + i) * 2 + 0) * 64 + lane) * 4);
    wop[i][1] = *reinterpret_cast<const u32x4*>(ldu + L_WO + (((6 * (wave & 3) + i) * 2 + 1) * 64 + lane) * 4);
  }
  __syncthreads();
  {
    const unsigned pbase = WS_P + (unsigned)(team + 8 * ((2 * b + ((wave >> 1) & 1)) * 8 + (wave >> 2) * 4)) * 1024;
    const unsigned po = (lane < 48 ? (wave & 1) * 48 + lane : 96) * 8;
    u32x2 go[GS];
    unsigned pend = (1u << GS) - 1u, spins = 0;
    do {
      MEMBAR();
#pragma unroll
      for (int s = 0; s < GS; ++s)
        if (pend & (1u << s)) go[s] = LD64(rw, po, pbase + s * (8 * 1024), AUX_SC1);
#pragma unroll
      for (int s = 0; s < GS; ++s)
        if (pend & (1u << s)) { if (__all((go[s].x & go[s].y & 1u) != 0u)) pend &= ~(1u << s); }
      if (PSLEEP && pend != 0u) __builtin_amdgcn_s_sleep(PSLEEP);
    } while (pend != 0u && ++spins < SPIN_CAP);
    const int sl = lane & 3;
    const u32x2 gm = sl == 0 ? go[0] : sl == 1 ? go[1] : sl == 2 ? go[2] : go[3];
    const float ms = __uint_as_float(gm.x);
    float M = fmaxf(ms, dpp<XOR1>(ms)); M = fmaxf(M, dpp<XOR2>(M));
    const float f = __builtin_amdgcn_exp2f(ms - M);
    float Ls = f * __uint_as_float(gm.y);
    Ls += dpp<XOR1>(Ls); Ls += dpp<XOR2>(Ls);
    const float wgt = f * __builtin_amdgcn_rcpf(Ls);
    f32x2 oa = splat(rdlane(wgt, 48)) * asf2(go[0].x, go[0].y), ob = splat(rdlane(wgt, 49)) * asf2(go[1].x, go[1].y);
    oa = fma2(splat(rdlane(wgt, 50)), asf2(go[2].x, go[2].y), oa); ob = fma2(splat(rdlane(wgt, 51)), asf2(go[3].x, go[3].y), ob);
    oa += ob;
    if (lane < 48) ldu[L_O + 48 * wave + lane] = pack_bf16(oa.x, oa.y);
  }
  STAMP(4);
  {
    unsigned op[6];
#pragma unroll
    for (int i = 0; i < 6; ++i) op[i] = ldu[L_O + 48 * wave + 8 * i + r];
    float y[8];
#pragma unroll
    for (int k = 0; k < 8; ++k) y[k] = 0.f;
#pragma unroll
    for (int i = 0; i < 6; ++i) {
#pragma unroll
      for (int k = 0; k < 4; ++k) { y[k] = dot2(wop[i][0][k], op[i], y[k]); y[k + 4] = dot2(wop[i][1][k], op[i], y[k + 4]); }
    }
    const float z0 = fold32(y[0], y[4]), z1 = fold32(y[1], y[5]), z2 = fold32(y[2], y[6]), z3 = fold32(y[3], y[7]);
    const float u = fold8(fold16(z0, z2), fold16(z1, z3), bit3);
    if (c < 5) lds[L_YR + wave * 40 + fidx] = u;
  }
  __syncthreads();
  if (tid < 40) {
    const int bb = tid >= 20, t = tid - 20 * bb;
    f32x2 v[4];
#pragma unroll
    for (int w = 0; w < 4; ++w) v[w] = *reinterpret_cast<const f32x2*>(lds + L_YR + (4 * bb + w) * 40 + 2 * t);
    const f32x2 ys = (v[0] + v[1]) + (v[2] + v[3]);
    ST64((u32x2{vbit(ys.x), vbit(ys.y)}), rw, (bb * 8 * 320 + t) * 16, WS_Y + (team * 320 + cb * 20) * 16 + b * 8);
  }
  STAMP(5);
  __syncthreads();
  if (wave >= 3) {
    u32x4 gy[8];
    const unsigned yo = (tid - 192) * 16, ybase = WS_Y + b * (8 * 320 * 16);
    unsigned pend = 0xffu, spins = 0;
    do {
      MEMBAR();
#pragma unroll
      for (int s = 0; s < 8; ++s)
        if (pend & (1u << s)) gy[s] = LD128(rw, yo, ybase + s * (320 * 16), AUX_SC1);
#pragma unroll
      for (int s = 0; s < 8; ++s)
        if (pend & (1u << s)) { if (__all((gy[s].x & gy[s].y & gy[s].z & gy[s].w & 1u) != 0u)) pend &= ~(1u << s); }
      if (PSLEEP && pend != 0u) __builtin_amdgcn_s_sleep(PSLEEP);
    } while (pend != 0u && ++spins < SPIN_CAP);
    f32x2 ap[8];
#pragma unroll
    for (int s = 0; s < 8; ++s) ap[s] = asf2(gy[s].x, gy[s].y) + asf2(gy[s].z, gy[s].w);
    const f32x2 a = ((ap[0] + ap[1]) + (ap[2] + ap[3])) + ((ap[4] + ap[5]) + (ap[6] + ap[7]));
    const unsigned hp = pack_bf16(bflo(xres) + rbf(a.x), bfhi(xres) + rbf(a.y));
    ldu[L_H + tid - 192] = hp;
    float st = fold32(dot2(hp, ONES, 0.f), dot2(hp, hp, 0.f));
    st = row_sum16(fold16(st, st));
    if ((lane & 31) == 0) lds[L_ST + (wave - 3) * 2 + (lane >> 5)] = st;
  }
  __syncthreads();
  STAMP(6);
  const int d0 = 5 * (team * 16 + cb);
  float he;
  {
    const int de = d0 + (lane < 5 ? lane : 0);
    const unsigned hw = ldu[L_H + (de >> 1)];
    f32x2 sv[5];
#pragma unroll
    for (int w = 0; w < 5; ++w) sv[w] = *reinterpret_cast<const f32x2*>(lds + L_ST + 2 * w);
    const f32x2 st = (((sv[0] + sv[1]) + (sv[2] + sv[3])) + sv[4]) * splat(1.0f / D);
    he = (de & 1) ? bfhi(hw) : bflo(hw);
    const float mean1 = st.x;
    const float var1 = fmaxf(fmaf(-mean1, mean1, st.y), 0.f);
    const float i1 = ln_den_inv(var1), m1 = rbf(mean1);
    const float m2 = rbf((mean1 - m1) * i1), i2 = ln_den_inv(var1 * i1 * i1);
    const float tl = rbf(rbf(rbf(rbf(he - m1) * i1) - m2) * i2);
    constexpr float G1 = -2.0f * 0.7978845608028654f * LOG2E, G3 = G1 * 0.044715f;
    float te[5];
#pragma unroll
    for (int e = 0; e < 5; ++e) te[e] = rdlane(tl, e);
    f32x2 u[8], z[8];
#pragma unroll
    for (int e = 0; e < 5; ++e) u[e] = fma2(splat(te[e]), fw1, fb1);
    u[5] = fma2(f32x2{te[0], te[1]}, splat(gw1), splat(gb1));
    u[6] = fma2(f32x2{te[2], te[3]}, splat(gw1), splat(gb1));
    u[7] = fma2(splat(te[4]), splat(gw1), splat(gb1));
    SB();
#pragma unroll
    for (int k = 0; k < 8; ++k) z[k] = u[k] * u[k];
    SB();
#pragma unroll
    for (int k = 0; k < 8; ++k) z[k] = fma2(z[k], splat(G3), splat(G1));
    SB();
#pragma unroll
    for (int k = 0; k < 8; ++k) z[k] = z[k] * u[k];
    SB();
#pragma unroll
    for (int k = 0; k < 8; ++k) { z[k].x = __builtin_amdgcn_exp2f(z[k].x); if (k < 7) z[k].y = __builtin_amdgcn_exp2f(z[k].y); }
#pragma unroll
    for (int k = 0; k < 8; ++k) u[k] = u[k] * (k < 5 ? fw2 : splat(gw2));
    SB();
#pragma unroll
    for (int k = 0; k < 8; ++k) z[k] = z[k] + splat(1.0f);
    SB();
#pragma unroll
    for (int k = 0; k < 8; ++k) { z[k].x = __builtin_amdgcn_rcpf(z[k].x); if (k < 7) z[k].y = __builtin_amdgcn_rcpf(z[k].y); }
    SB();
#pragma unroll
    for (int k = 0; k < 8; ++k) u[k] = u[k] * z[k];
    const float ya0 = (u[0].x + u[0].y) + u[5].x, ya1 = (u[1].x + u[1].y) + u[5].y, ya2 = (u[2].x + u[2].y) + u[6].x,
                ya3 = (u[3].x + u[3].y) + u[6].y, ya4 = (u[4].x + u[4].y) + u[7].x;
    const float f4 = fold32(ya4, ya4);
    float r0 = fold8(fold16(fold32(ya0, ya1), fold32(ya2, ya3)), fold16(f4, f4), bit3);
    r0 += dpp<XOR1>(r0); r0 += dpp<XOR2>(r0); r0 += dpp<HMIR>(r0);
    const int el = bit3 ? 4 : (lane >> 5) + 2 * ((lane >> 4) & 1);
    if ((lane & 15) == 0 || lane == 8) lds[L_MR + el * 8 + wave] = r0;
  }
  __syncthreads();
  if (tid < 5) {
    typedef float f32x4 __attribute__((ext_vector_type(4)));
    const f32x4 pa = *reinterpret_cast<const f32x4*>(lds + L_MR + tid * 8), pb = *reinterpret_cast<const f32x4*>(lds + L_MR + tid * 8 + 4);
    const f32x4 ps = pa + pb;
    const float yv = (ps.x + ps.y) + (ps.z + ps.w);
    const unsigned o = pack_bf16(he + rbf(rbf(yv) + fb2), 0.f);
    __builtin_amdgcn_raw_buffer_store_b16((unsigned short)o, __builtin_amdgcn_make_buffer_rsrc((void*)out, 0, 2 * D * 2 + 4, RSRC3),
                                          tid * 2, (b * D + d0) * 2, 0);
  }
  if (tid == 0 && bid == 0)
    __builtin_amdgcn_raw_buffer_store_b64(u32x2{tlo, thi}, rw, 0, WS_EPOCH, AUX_SC1);
  STAMP(7);
}

}

extern "C" void kernel_launch(void* const* d_in, const int* in_sizes, int n_in, void* d_out, int out_size, void* d_ws,
                              size_t ws_size, hipStream_t stream) {
  (void)in_sizes; (void)n_in; (void)out_size; (void)ws_size;
  fused_kernel<<<dim3(NWG), dim3(NTHR), 0, stream>>>(
      (const char*)d_in[0], (const char*)d_in[1], (const char*)d_in[2], (const char*)d_in[3], (const char*)d_in[4],
      (const char*)d_in[6], (const char*)d_in[7], (const char*)d_in[8], (const char*)d_in[9], (char*)d_out, (char*)d_ws);
}
```

```cpp
#include <hip/hip_runtime.h>
#include <hip/hip_bf16.h>
#include <stdint.h>
#include <type_traits>

namespace {

typedef unsigned u32x4 __attribute__((ext_vector_type(4)));
typedef unsigned u32x2 __attribute__((ext_vector_type(2)));
typedef float f32x2 __attribute__((ext_vector_type(2)));
typedef __bf16 bf16x2 __attribute__((ext_vector_type(2)));

constexpr int D = 640, H = 32, SK = 8192, DH = 192, FF = 1536;
constexpr int NWG = 256, NTHR = 512, NWAVE = 8;
constexpr unsigned TILE_BYTES = 8 * DH * 2;
constexpr unsigned HEAD_BYTES = SK * DH * 2;
#define NOQ 0
#define NOWO 0
#define RA 8
#define PSLEEP 0
#define CATCH 3
#define RPRE 5
constexpr int GS = 4;
constexpr int NSW = 4, NH = NWAVE - NSW;
constexpr int STEPS = SK / 8 / (GS * NSW);
constexpr unsigned STRIDE = GS * NSW * TILE_BYTES;
constexpr int AUX_NT = 2, AUX_SC1 = 16;
constexpr unsigned RSRC3 = 0x00020000u;
constexpr unsigned OOB = 0x80000000u;

constexpr unsigned WS_EPOCH = 0;
constexpr unsigned WS_RDY = 4096;
constexpr unsigned WS_P = 8192;
constexpr unsigned WS_Y = WS_P + NWG * 1024;
constexpr unsigned WS_QP = WS_Y + 2 * 8 * 320 * 16;
constexpr unsigned WS_END = WS_QP + 32 * 2 * 8 * 96 * 16;

constexpr int L_RED = 0;
constexpr int L_ML = L_RED + NSW * DH;
constexpr int L_Q = L_ML + 16;
constexpr int L_TAG = L_Q + 96;
constexpr int L_O = L_TAG + 4;
constexpr int L_YR = L_O + 384;
constexpr int L_H = L_YR + 320;
constexpr int L_ST = L_H + 320;
constexpr int L_MR = L_ST + 16;
constexpr int L_WO = L_MR + 64;
constexpr int L_TOTAL = 24 * 1024;
static_assert(L_WO + 24 * 2 * 64 * 4 <= L_TOTAL, "lds");
static_assert(L_WO % 4 == 0 && L_Q % 4 == 0 && L_ST % 4 == 0 && L_MR % 4 == 0, "alignment");

__device__ __forceinline__ float dot2(unsigned a, unsigned b, float c) {
  return __builtin_amdgcn_fdot2_f32_bf16(__builtin_bit_cast(bf16x2, a), __builtin_bit_cast(bf16x2, b), c, false);
}
template <int CTRL> __device__ __forceinline__ float dpp(float x) {
  return __builtin_bit_cast(float, __builtin_amdgcn_mov_dpp(__builtin_bit_cast(int, x), CTRL, 0xf, 0xf, true));
}
constexpr int XOR1 = 0xB1, XOR2 = 0x4E, HMIR = 0x141, ROR8 = 0x128;
constexpr unsigned ONES = 0x3f803f80u;
__device__ __forceinline__ float bflo(unsigned u) { return __builtin_bit_cast(float, u << 16); }
__device__ __forceinline__ float bfhi(unsigned u) { return __builtin_bit_cast(float, u & 0xffff0000u); }
__device__ __forceinline__ f32x2 bf2(unsigned u) { return f32x2{bflo(u), bfhi(u)}; }
__device__ __forceinline__ unsigned pack_bf16(float lo, float hi) {
  bf16x2 v; v[0] = (__bf16)lo; v[1] = (__bf16)hi;
  return __builtin_bit_cast(unsigned, v);
}
__device__ __forceinline__ float rbf(float x) {
  typedef float f32x2_ __attribute__((ext_vector_type(2)));
  return __builtin_bit_cast(float, __builtin_convertvector(f32x2_{0.f, x}, bf16x2));
}
__device__ __forceinline__ float fold32(float a, float b) {
  auto r = __builtin_amdgcn_permlane32_swap(__float_as_uint(a), __float_as_uint(b), false, false);
  return __uint_as_float(r[0]) + __uint_as_float(r[1]);
}
__device__ __forceinline__ float fold16(float a, float b) {
  auto r = __builtin_amdgcn_permlane16_swap(__float_as_uint(a), __float_as_uint(b), false, false);
  return __uint_as_float(r[0]) + __uint_as_float(r[1]);
}
__device__ __forceinline__ float fold8(float a, float b, bool bit3) {
  const float keep = bit3 ? b : a, send = bit3 ? a : b;
  return keep + dpp<ROR8>(send);
}
__device__ __forceinline__ float max3216(float x) {
  auto s = __builtin_amdgcn_permlane16_swap(__float_as_uint(x), __float_as_uint(x), false, false);
  x = fmaxf(__uint_as_float(s[0]), __uint_as_float(s[1]));
  auto t = __builtin_amdgcn_permlane32_swap(__float_as_uint(x), __float_as_uint(x), false, false);
  return fmaxf(__uint_as_float(t[0]), __uint_as_float(t[1]));
}
__device__ __forceinline__ float row_sum16(float x) {
  x += dpp<XOR1>(x); x += dpp<XOR2>(x); x += dpp<HMIR>(x); x += dpp<ROR8>(x);
  return x;
}
__device__ __forceinline__ unsigned long long mix64(unsigned long long z) {
  z += 0x9E3779B97F4A7C15ull;
  z = (z ^ (z >> 30)) * 0xBF58476D1CE4E5B9ull;
  z = (z ^ (z >> 27)) * 0x94D049BB133111EBull;
  return z ^ (z >> 31);
}
__device__ __forceinline__ u32x4 pack2(float a, float b, unsigned t8, unsigned thi) {
  u32x4 r; r.x = (__float_as_uint(a) >> 8) | t8; r.y = thi; r.z = (__float_as_uint(b) >> 8) | t8; r.w = thi; return r;
}
__device__ __forceinline__ unsigned tag_bad(u32x4 g, unsigned t8, unsigned thi) {
  return (((g.x ^ t8) | (g.z ^ t8)) >> 24) | (g.y ^ thi) | (g.w ^ thi);
}
__device__ __forceinline__ float g0(u32x4 g) { return __uint_as_float(g.x << 8); }
__device__ __forceinline__ float g1(u32x4 g) { return __uint_as_float(g.z << 8); }
__device__ __forceinline__ f32x2 g01(u32x4 g) { return f32x2{g0(g), g1(g)}; }
__device__ __forceinline__ unsigned vbit(float x) { return __float_as_uint(x) | 1u; }
__device__ __forceinline__ f32x2 asf2(unsigned a, unsigned b) { return f32x2{__uint_as_float(a), __uint_as_float(b)}; }
__device__ __forceinline__ float ln_den_inv(float var) {
  return __builtin_amdgcn_rcpf(rbf(__builtin_amdgcn_sqrtf(rbf(rbf(var) + 1e-5f))));
}
__device__ __forceinline__ float rdlane(float x, int l) { return __uint_as_float(__builtin_amdgcn_readlane(__float_as_uint(x), l)); }
__device__ __forceinline__ f32x2 splat(float x) { return f32x2{x, x}; }
__device__ __forceinline__ f32x2 fma2(f32x2 a, f32x2 b, f32x2 c) { return __builtin_elementwise_fma(a, b, c); }

#define SB() __builtin_amdgcn_sched_barrier(0)
#define MEMBAR() asm volatile("" ::: "memory")
#define LD128(rs, vo, so, aux) __builtin_amdgcn_raw_buffer_load_b128((rs), (vo), (so), (aux))
#define ST128(v, rs, vo, so) __builtin_amdgcn_raw_buffer_store_b128((v), (rs), (vo), (so), AUX_SC1)
#define LD64(rs, vo, so, aux) __builtin_amdgcn_raw_buffer_load_b64((rs), (vo), (so), (aux))
#define ST64(v, rs, vo, so) __builtin_amdgcn_raw_buffer_store_b64((v), (rs), (vo), (so), AUX_SC1)
#define VSPLIT 64
#define KSPLIT 56
#define WAIT_VM3() __builtin_amdgcn_s_waitcnt(0x0F73)
#define STAMP(i) do { } while (0)

constexpr unsigned SPIN_CAP = 1u << 17;

__global__ void __launch_bounds__(NTHR, 2)
fused_kernel(const char* x, const char* kc, const char* vc, const char* wq, const char* wo, const char* w1,
             const char* b1, const char* w2, const char* b2, char* out, char* ws) {
  __shared__ __attribute__((aligned(16))) float lds[L_TOTAL];
  unsigned* const ldu = reinterpret_cast<unsigned*>(lds);
  const int tid = threadIdx.x, lane = tid & 63, wave = __builtin_amdgcn_readfirstlane(tid >> 6);
  const int bid = blockIdx.x, team = bid & 7, jj = bid >> 3, hl = jj >> 3, b = (jj >> 2) & 1, sp = jj & 3;
  const int h = team * 4 + hl, cb = hl * 4 + sp, pq = jj & 7;
  const int r = lane >> 3, c = lane & 7;
  const bool bit3 = (lane & 8) != 0;
  const int fidx = 8 * c + 4 * (lane >> 5) + 2 * ((lane >> 4) & 1) + (bit3 ? 1 : 0);
  STAMP(0);
  asm volatile("" : "+s"(x), "+s"(kc), "+s"(vc), "+s"(wq), "+s"(wo), "+s"(w1), "+s"(b1), "+s"(w2), "+s"(b2), "+s"(out), "+s"(ws));
  const __amdgpu_buffer_rsrc_t rw = __builtin_amdgcn_make_buffer_rsrc((void*)ws, 0, WS_END, RSRC3);
  const __amdgpu_buffer_rsrc_t rx = __builtin_amdgcn_make_buffer_rsrc((void*)x, 0, 2 * D * 2, RSRC3);
  constexpr float SCALE = 0.07216878364870322f, LOG2E = 1.4426950408889634f;
  unsigned vzero;
  asm volatile("v_mov_b32 %0, 0" : "=v"(vzero));

  f32x2 fw1, fb1, fw2; float gw1, gb1, gw2, fb2;
  unsigned xres;
#define LOAD_SMALL() \
    unsigned short a1[3], a2[3], a3[3], a4; { \
    const __amdgpu_buffer_rsrc_t r1 = __builtin_amdgcn_make_buffer_rsrc((void*)w1, 0, FF * 2 + 4, RSRC3); \
    const __amdgpu_buffer_rsrc_t r2 = __builtin_amdgcn_make_buffer_rsrc((void*)b1, 0, FF * 2 + 4, RSRC3); \
    const __amdgpu_buffer_rsrc_t r3 = __builtin_amdgcn_make_buffer_rsrc((void*)w2, 0, FF * 2 + 4, RSRC3); \
    _Pragma("unroll") for (int k = 0; k < 3; ++k) { \
      a1[k] = __builtin_amdgcn_raw_buffer_load_b16(r1, tid * 2 + 1024 * k, 0, 0); \
      a2[k] = __builtin_amdgcn_raw_buffer_load_b16(r2, tid * 2 + 1024 * k, 0, 0); \
      a3[k] = __builtin_amdgcn_raw_buffer_load_b16(r3, tid * 2 + 1024 * k, 0, 0); \
    } \
    a4 = __builtin_amdgcn_raw_buffer_load_b16(__builtin_amdgcn_make_buffer_rsrc((void*)b2, 0, 4, RSRC3), vzero, 0, 0); \
    xres = __builtin_amdgcn_raw_buffer_load_b32(rx, tid >= 192 ? (tid - 192) * 4 : OOB, b * D * 2, 0); \
    MEMBAR(); }
#define CONV_SMALL() { \
    fw1 = f32x2{bflo(a1[0]), bflo(a1[1])}; fb1 = f32x2{bflo(a2[0]), bflo(a2[1])}; fw2 = f32x2{bflo(a3[0]), bflo(a3[1])}; \
    gw1 = bflo(a1[2]); gb1 = bflo(a2[2]); gw2 = bflo(a3[2]); fb2 = bflo(a4); \
    asm volatile("" : "+v"(fw1), "+v"(fb1), "+v"(fw2), "+v"(gw1), "+v"(gb1), "+v"(gw2), "+v"(fb2), "+v"(xres) :: "memory"); }

  if (wave >= NH) {
   auto stream = [&](auto kaux_, auto vaux_) __attribute__((always_inline)) {
    constexpr int KAUX = decltype(kaux_)::value, VAUX = decltype(vaux_)::value;
    const int sw = wave - NH;
    const size_t kvbase = (size_t)(b * H + h) * HEAD_BYTES;
    const __amdgpu_buffer_rsrc_t rk = __builtin_amdgcn_make_buffer_rsrc((void*)(kc + kvbase), 0, HEAD_BYTES, RSRC3);
    const __amdgpu_buffer_rsrc_t rv = __builtin_amdgcn_make_buffer_rsrc((void*)(vc + kvbase), 0, HEAD_BYTES, RSRC3);
    const unsigned kvo = r * (DH * 2) + c * 16;
    u32x4 kbuf[RPRE][3], vbuf[RPRE][3];
    unsigned so = (sp * NSW + sw) * TILE_BYTES + (h & 1) * RA * STRIDE;
    static_assert(RA + RPRE + 2 < STEPS, "no wrap before the main loop");
#pragma unroll
    for (int j = 0; j < RPRE; ++j) {
      if (j) { WAIT_VM3(); SB(); }
#pragma unroll
      for (int t = 0; t < 3; ++t) { kbuf[j][t] = LD128(rk, kvo + t * 128, so, KAUX); SB(); }
      if (j) { WAIT_VM3(); SB(); }
#pragma unroll
      for (int t = 0; t < 3; ++t) { vbuf[j][t] = LD128(rv, kvo + t * 128, so, VAUX); SB(); }
      so += STRIDE;
    }
    LOAD_SMALL();
    STAMP(8);
    __syncthreads();
    u32x4 q[3];
#pragma unroll
    for (int t = 0; t < 3; ++t) q[t] = *reinterpret_cast<const u32x4*>(ldu + L_Q + (8 * t + c) * 4);
    STAMP(1);
    float m = -INFINITY, l = 0.f;
    f32x2 acc[12];
#pragma unroll
    for (int k = 0; k < 12; ++k) acc[k] = f32x2{0.f, 0.f};

#define PIN12(A) asm volatile("" : "+v"(A[0]), "+v"(A[1]), "+v"(A[2]), "+v"(A[3]), "+v"(A[4]), "+v"(A[5]), "+v"(A[6]), "+v"(A[7]), \
    "+v"(A[8]), "+v"(A[9]), "+v"(A[10]), "+v"(A[11]) :: "memory")
#define STEP(KB, VB, REFILL) { \
    float s0_ = 0.f, s1_ = 0.f, s2_ = 0.f; \
    _Pragma("unroll") for (int j_ = 0; j_ < 4; ++j_) { s0_ = dot2(KB[0][j_], q[0][j_], s0_); s1_ = dot2(KB[1][j_], q[1][j_], s1_); s2_ = dot2(KB[2][j_], q[2][j_], s2_); } \
    asm volatile("" : "+v"(s0_), "+v"(s1_), "+v"(s2_) :: "memory"); \
    SB(); \
    if (REFILL) { _Pragma("unroll") for (int u_ = 0; u_ < 3; ++u_) KB[u_] = LD128(rk, kvo + u_ * 128, so, KAUX); } \
    SB(); \
    float s_ = s0_ + s1_ + s2_; \
    s_ += dpp<XOR1>(s_); s_ += dpp<XOR2>(s_); s_ += dpp<HMIR>(s_); \
    s_ = rbf(rbf(s_) * SCALE) * LOG2E; \
    if (__any(s_ > m)) { \
      const float mn_ = fmaxf(m, s_); \
      const float al_ = __builtin_amdgcn_exp2f(m - mn_); \
      m = mn_; l *= al_; \
      _Pragma("unroll") for (int k_ = 0; k_ < 12; ++k_) acc[k_] *= splat(al_); \
    } \
    float p_ = __builtin_amdgcn_exp2f(s_ - m); \
    asm volatile("" : "+v"(p_)); \
    SB(); \
    f32x2 vf_[12]; \
    _Pragma("unroll") for (int t_ = 0; t_ < 3; ++t_) \
      _Pragma("unroll") for (int j_ = 0; j_ < 4; ++j_) vf_[t_ * 4 + j_] = bf2(VB[t_][j_]); \
    PIN12(vf_); \
    SB(); \
    if (REFILL) { _Pragma("unroll") for (int u_ = 0; u_ < 3; ++u_) VB[u_] = LD128(rv, kvo + u_ * 128, so, VAUX); } \
    SB(); \
    l += p_; \
    _Pragma("unroll") for (int k_ = 0; k_ < 12; ++k_) acc[k_] = fma2(splat(p_), vf_[k_], acc[k_]); \
    PIN12(acc); \
    SB(); }

#define STEP2(KB, VB, KN, VN) { \
    __builtin_amdgcn_s_waitcnt(0x0F73); \
    SB(); \
    _Pragma("unroll") for (int u_ = 0; u_ < 3; ++u_) KN[u_] = LD128(rk, kvo + u_ * 128, so, KAUX); \
    MEMBAR(); \
    SB(); \
    float s0_ = 0.f, s1_ = 0.f, s2_ = 0.f; \
    _Pragma("unroll") for (int j_ = 0; j_ < 4; ++j_) { s0_ = dot2(KB[0][j_], q[0][j_], s0_); s1_ = dot2(KB[1][j_], q[1][j_], s1_); s2_ = dot2(KB[2][j_], q[2][j_], s2_); } \
    float s_ = s0_ + s1_ + s2_; \
    s_ += dpp<XOR1>(s_); s_ += dpp<XOR2>(s_); s_ += dpp<HMIR>(s_); \
    s_ = rbf(rbf(s_) * SCALE) * LOG2E; \
    if (__any(s_ > m)) { \
      const float mn_ = fmaxf(m, s_); \
      const float al_ = __builtin_amdgcn_exp2f(m - mn_); \
      m = mn_; l *= al_; \
      _Pragma("unroll") for (int k_ = 0; k_ < 12; ++k_) acc[k_] *= splat(al_); \
    } \
    float p_ = __builtin_amdgcn_exp2f(s_ - m); \
    asm volatile("" : "+v"(p_) :: "memory"); \
    SB(); \
    __builtin_amdgcn_s_waitcnt(0x0F73); \
    SB(); \
    _Pragma("unroll") for (int u_ = 0; u_ < 3; ++u_) VN[u_] = LD128(rv, kvo + u_ * 128, so, VAUX); \
    MEMBAR(); \
    SB(); \
    l += p_; \
    _Pragma("unroll") for (int t_ = 0; t_ < 3; ++t_) \
      _Pragma("unroll") for (int j_ = 0; j_ < 4; ++j_) acc[t_ * 4 + j_] = fma2(splat(p_), bf2(VB[t_][j_]), acc[t_ * 4 + j_]); \
    PIN12(acc); \
    SB(); }

    static_assert(RPRE % 2 == 1 && CATCH >= 1 && CATCH < RPRE, "parity of the main loop");
    STEP(kbuf[0], vbuf[0], true);
#pragma unroll
    for (int jb = 1; jb <= CATCH; ++jb) STEP(kbuf[jb], vbuf[jb], false);
    so += STRIDE;
    WAIT_VM3(); SB();
#pragma unroll
    for (int t = 0; t < 3; ++t) kbuf[1][t] = LD128(rk, kvo + t * 128, so, KAUX);
    SB(); WAIT_VM3(); SB();
#pragma unroll
    for (int t = 0; t < 3; ++t) vbuf[1][t] = LD128(rv, kvo + t * 128, so, VAUX);
    MEMBAR(); SB();
#pragma unroll
    for (int jb = CATCH + 1; jb < RPRE; ++jb) STEP(kbuf[jb], vbuf[jb], false);
    STEP(kbuf[0], vbuf[0], false);
    CONV_SMALL();
#pragma nounroll
    for (int i = RPRE + 1; i < STEPS; i += 2) {
      so += STRIDE;
      if (so >= HEAD_BYTES) so -= HEAD_BYTES;
      STEP2(kbuf[1], vbuf[1], kbuf[0], vbuf[0]);
      so += STRIDE;
      if (so >= HEAD_BYTES) so -= HEAD_BYTES;
      if (i == STEPS - 2) so = OOB;
      STEP2(kbuf[0], vbuf[0], kbuf[1], vbuf[1]);
    }
    STAMP(2);
    {
      float mw = fmaxf(m, dpp<ROR8>(m));
      mw = max3216(mw);
      const float f = __builtin_amdgcn_exp2f(m - mw);
      float lw = l * f;
      lw += dpp<ROR8>(lw); lw = fold16(lw, lw); lw = fold32(lw, lw);
#pragma unroll
      for (int k = 0; k < 12; ++k) acc[k] *= splat(f);
      float* const red = lds + L_RED + sw * DH + fidx;
#pragma unroll
      for (int t = 0; t < 3; ++t) {
        const float z0 = fold32(acc[t * 4 + 0].x, acc[t * 4 + 2].x), z1 = fold32(acc[t * 4 + 0].y, acc[t * 4 + 2].y);
        const float z2 = fold32(acc[t * 4 + 1].x, acc[t * 4 + 3].x), z3 = fold32(acc[t * 4 + 1].y, acc[t * 4 + 3].y);
        red[64 * t] = fold8(fold16(z0, z2), fold16(z1, z3), bit3);
      }
      if (lane == 0) *reinterpret_cast<f32x2*>(lds + L_ML + 2 * sw) = f32x2{mw, lw};
    }
    __builtin_amdgcn_s_waitcnt(0x0F70);
   };
   using NT_ = std::integral_constant<int, AUX_NT>; using DF_ = std::integral_constant<int, 0>;
   const int bh = b * H + h;
   if (KSPLIT < 64 && bh >= KSPLIT) { if (bh >= VSPLIT) stream(DF_{}, DF_{}); else stream(DF_{}, NT_{}); }
   else if (VSPLIT < 64 && bh >= VSPLIT) stream(NT_{}, DF_{});
   else stream(NT_{}, NT_{});
  } else {
    if (wave < 3 && !NOQ) {
      const __amdgpu_buffer_rsrc_t rq = __builtin_amdgcn_make_buffer_rsrc((void*)wq, 0, D * H * DH * 2, RSRC3);
      unsigned xs[2][5], xp[2];
#pragma unroll
      for (int bb = 0; bb < 2; ++bb) {
#pragma unroll
        for (int i = 0; i < 5; ++i) xs[bb][i] = __builtin_amdgcn_raw_buffer_load_b32(rx, lane * 4 + i * 256 + bb * D * 2, 0, 0);
        xp[bb] = __builtin_amdgcn_raw_buffer_load_b32(rx, (c < 5 ? (8 * c + r) * 4 : 0) + bb * D * 2, pq * 160, 0);
      }
      const u32x2 ep = __builtin_amdgcn_raw_buffer_load_b64(rw, vzero, WS_EPOCH, AUX_SC1);
      SB();
      u32x4 wqr[5][2];
      {
        const unsigned vo = (unsigned)(2 * r) * (H * DH * 2) + c * 16;
        const unsigned qso = (unsigned)(80 * pq) * (H * DH * 2) + h * (DH * 2) + wave * 128;
#pragma unroll
        for (int i = 0; i < 5; ++i)
#pragma unroll
          for (int e = 0; e < 2; ++e) { wqr[i][e] = LD128(rq, vo, qso + (16 * i + e) * (H * DH * 2), AUX_NT); SB(); }
      }
      float s0 = 0.f, s1 = 0.f, q0 = 0.f, q1 = 0.f;
#pragma unroll
      for (int i = 0; i < 5; ++i) {
        s0 = dot2(xs[0][i], ONES, s0); q0 = dot2(xs[0][i], xs[0][i], q0);
        s1 = dot2(xs[1][i], ONES, s1); q1 = dot2(xs[1][i], xs[1][i], q1);
      }
      const float st = row_sum16(fold16(fold32(s0, s1), fold32(q0, q1)));
      auto sw2 = __builtin_amdgcn_permlane16_swap(__float_as_uint(st), __float_as_uint(st), false, false);
      const float mean = __uint_as_float(sw2[0]) * (1.0f / D);
      const float var = fmaxf(fmaf(-mean, mean, __uint_as_float(sw2[1]) * (1.0f / D)), 0.f);
      const float inv = ln_den_inv(var), mb = rbf(mean);
      const float m0 = rdlane(mb, 0), m1 = rdlane(mb, 32), i0 = rdlane(inv, 0), i1 = rdlane(inv, 32);
      const unsigned xn0 = pack_bf16(rbf(bflo(xp[0]) - m0) * i0, rbf(bfhi(xp[0]) - m0) * i0);
      const unsigned xn1 = pack_bf16(rbf(bflo(xp[1]) - m1) * i1, rbf(bfhi(xp[1]) - m1) * i1);
      const unsigned long long tg = mix64(((unsigned long long)(unsigned)__builtin_amdgcn_readfirstlane(ep.y) << 32) | (unsigned)__builtin_amdgcn_readfirstlane(ep.x));
      const unsigned thi = (unsigned)(tg >> 32), t8 = (unsigned)tg & 0xff000000u;
      float a0[8], a1q[8];
#pragma unroll
      for (int k = 0; k < 8; ++k) { a0[k] = 0.f; a1q[k] = 0.f; }
#define QGROUP(I) { \
        unsigned b0_ = __builtin_amdgcn_update_dpp(0u, xn0, 0x150 + I, 0xf, 0x3, false); b0_ = __builtin_amdgcn_update_dpp(b0_, xn0, 0x158 + I, 0xf, 0xc, false); \
        unsigned b1_ = __builtin_amdgcn_update_dpp(0u, xn1, 0x150 + I, 0xf, 0x3, false); b1_ = __builtin_amdgcn_update_dpp(b1_, xn1, 0x158 + I, 0xf, 0xc, false); \
        _Pragma("unroll") for (int j = 0; j < 4; ++j) { \
          const unsigned plo = __builtin_amdgcn_perm(wqr[I][1][j], wqr[I][0][j], 0x05040100u), phi = __builtin_amdgcn_perm(wqr[I][1][j], wqr[I][0][j], 0x07060302u); \
          a0[2 * j] = dot2(plo, b0_, a0[2 * j]); a0[2 * j + 1] = dot2(phi, b0_, a0[2 * j + 1]); \
          a1q[2 * j] = dot2(plo, b1_, a1q[2 * j]); a1q[2 * j + 1] = dot2(phi, b1_, a1q[2 * j + 1]); } }
      QGROUP(0) QGROUP(1) QGROUP(2) QGROUP(3) QGROUP(4)
      float z[8];
#pragma unroll
      for (int k = 0; k < 8; ++k) z[k] = fold32(a0[k], a1q[k]);
      float y[4];
#pragma unroll
      for (int k = 0; k < 4; ++k) y[k] = fold16(z[k], z[k + 4]);
      const float u0 = fold8(y[0], y[2], bit3), u1 = fold8(y[1], y[3], bit3);
      const unsigned pidx = 32 * wave + 4 * c + 2 * ((lane >> 4) & 1) + (bit3 ? 1 : 0);
      ST128(pack2(u0, u1, t8, thi), rw, ((lane >> 5) * 8 * 96 + pidx) * 16, WS_QP + (h * 16 + pq) * (96 * 16));
      STAMP(8);
      asm volatile("s_waitcnt vmcnt(0)" ::: "memory");
      STAMP(10);
      {
        const unsigned qo = ((lane >> 5) * 96 + 32 * wave + (lane & 31)) * 16;
        const unsigned qb = WS_QP + (h * 16 + b * 8) * (96 * 16);
        u32x4 gq[4];
        for (unsigned spins = 0;; ++spins) {
          MEMBAR();
#pragma unroll
          for (int k = 0; k < 4; ++k) gq[k] = LD128(rw, qo, qb + k * (2 * 96 * 16), AUX_SC1);
          unsigned bad = 0;
#pragma unroll
          for (int k = 0; k < 4; ++k) bad |= tag_bad(gq[k], t8, thi);
          if (__all(bad == 0u) || spins > SPIN_CAP) break;
        }
        const f32x2 qq = (g01(gq[0]) + g01(gq[1])) + (g01(gq[2]) + g01(gq[3]));
        const float qa = fold32(qq.x, qq.x), qc = fold32(qq.y, qq.y);
        if (lane < 32) ldu[L_Q + 32 * wave + lane] = pack_bf16(qa, qc);
      }
      if (tid == 0) *reinterpret_cast<u32x2*>(ldu + L_TAG) = u32x2{(unsigned)tg, thi};
    }
    const unsigned yoff = ((lane >= 20 ? 8 * 320 : 0) + (lane >= 20 ? lane - 20 : lane)) * 16;
    const unsigned ybo = WS_Y + (team * 320 + cb * 20) * 16 + b * 8;
    if (wave == 3) {
      const u32x2 ep = LD64(rw, vzero, WS_EPOCH, AUX_SC1);
      ST128((u32x4{0u, 0u, 0u, 0u}), rw, lane * 16, WS_P + bid * 1024);
      ST64((u32x2{0u, 0u}), rw, lane < 40 ? yoff : OOB, ybo);
      asm volatile("s_waitcnt vmcnt(0)" ::: "memory");
      const unsigned long long tg = mix64(((unsigned long long)(unsigned)__builtin_amdgcn_readfirstlane(ep.y) << 32) | (unsigned)__builtin_amdgcn_readfirstlane(ep.x));
      ST64((u32x2{(unsigned)tg, (unsigned)(tg >> 32)}), rw, lane == 0 ? 0u : OOB, WS_RDY + bid * 8);
    }
    if (NOQ && wave == 0) {
      const u32x2 ep = __builtin_amdgcn_raw_buffer_load_b64(rw, vzero, WS_EPOCH, AUX_SC1);
      const unsigned long long tg = mix64(((unsigned long long)(unsigned)__builtin_amdgcn_readfirstlane(ep.y) << 32) | (unsigned)__builtin_amdgcn_readfirstlane(ep.x));
      if (tid == 0) *reinterpret_cast<u32x2*>(ldu + L_TAG) = u32x2{(unsigned)tg, (unsigned)(tg >> 32)};
      ldu[L_Q + lane] = 0x3c003c00; ldu[L_Q + 32 + lane] = 0x3c003c00;
    }
    STAMP(9);
    __syncthreads();
    STAMP(1);
    LOAD_SMALL();
    const __amdgpu_buffer_rsrc_t ro = __builtin_amdgcn_make_buffer_rsrc((void*)wo, 0, D * H * DH * 2, RSRC3);
    const unsigned vo = c < 5 ? (unsigned)(2 * r) * (D * 2) + c * 16 : OOB;
#pragma nounroll
    for (int g = wave; g < (NOWO ? 0 : 24); g += NH) {
      const unsigned wso = (unsigned)(768 * team + 384 * b + 16 * g) * (D * 2) + cb * 80;
      const u32x4 e0 = LD128(ro, vo, wso, AUX_NT);
      const u32x4 e1 = LD128(ro, vo + D * 2, wso, AUX_NT);
      u32x4 p0, p1;
      p0.x = __builtin_amdgcn_perm(e1.x, e0.x, 0x05040100u); p0.y = __builtin_amdgcn_perm(e1.x, e0.x, 0x07060302u);
      p0.z = __builtin_amdgcn_perm(e1.y, e0.y, 0x05040100u); p0.w = __builtin_amdgcn_perm(e1.y, e0.y, 0x07060302u);
      p1.x = __builtin_amdgcn_perm(e1.z, e0.z, 0x05040100u); p1.y = __builtin_amdgcn_perm(e1.z, e0.z, 0x07060302u);
      p1.z = __builtin_amdgcn_perm(e1.w, e0.w, 0x05040100u); p1.w = __builtin_amdgcn_perm(e1.w, e0.w, 0x07060302u);
      *reinterpret_cast<u32x4*>(ldu + L_WO + ((g * 2 + 0) * 64 + lane) * 4) = p0;
      *reinterpret_cast<u32x4*>(ldu + L_WO + ((g * 2 + 1) * 64 + lane) * 4) = p1;
    }
    CONV_SMALL();
    if (wave == 0) {
      const u32x2 tg = *reinterpret_cast<const u32x2*>(ldu + L_TAG);
      for (unsigned spins = 0; spins < SPIN_CAP; ++spins) {
        MEMBAR();
        const u32x4 r0 = LD128(rw, lane * 16, WS_RDY, AUX_SC1), r1 = LD128(rw, lane * 16 + 1024, WS_RDY, AUX_SC1);
        const unsigned bad = (r0.x ^ tg.x) | (r0.y ^ tg.y) | (r0.z ^ tg.x) | (r0.w ^ tg.y) | (r1.x ^ tg.x) | (r1.y ^ tg.y) | (r1.z ^ tg.x) | (r1.w ^ tg.y);
        if (__all(bad == 0u)) break;
        __builtin_amdgcn_s_sleep(16);
      }
    }
  }
  const u32x2 tg2 = *reinterpret_cast<const u32x2*>(ldu + L_TAG);
  const unsigned tlo = __builtin_amdgcn_readfirstlane(tg2.x), thi = __builtin_amdgcn_readfirstlane(tg2.y);

  __syncthreads();
  if (tid < 97) {
    const int d2 = tid < 96 ? 2 * tid : 0;
    f32x2 ml[NSW], v[NSW];
#pragma unroll
    for (int w = 0; w < NSW; ++w) {
      ml[w] = *reinterpret_cast<const f32x2*>(lds + L_ML + 2 * w);
      v[w] = *reinterpret_cast<const f32x2*>(lds + L_RED + w * DH + d2);
    }
    const float M = __uint_as_float(vbit(fmaxf(fmaxf(ml[0].x, ml[1].x), fmaxf(ml[2].x, ml[3].x))));
    float Ls = 0.f; f32x2 o = {0.f, 0.f};
#pragma unroll
    for (int w = 0; w < NSW; ++w) {
      const float f = __builtin_amdgcn_exp2f(ml[w].x - M);
      Ls = fmaf(f, ml[w].y, Ls); o = fma2(splat(f), v[w], o);
    }
    if (tid == 96) o = f32x2{M, Ls};
    ST64((u32x2{vbit(o.x), vbit(o.y)}), rw, tid * 8, WS_P + bid * 1024);
  }
  STAMP(3);
  u32x4 wop[6][2];
#pragma unroll
  for (int i = 0; i < 6; ++i) {
    wop[i][0] = *reinterpret_cast<const u32x4*>(ldu + L_WO + (((6 * (wave & 3) + i) * 2 + 0) * 64 + lane) * 4);
    wop[i][1] = *reinterpret_cast<const u32x4*>(ldu + L_WO + (((6 * (wave & 3) + i) * 2 + 1) * 64 + lane) * 4);
  }
  __syncthreads();
  {
    const unsigned pbase = WS_P + (unsigned)(team + 8 * ((2 * b + ((wave >> 1) & 1)) * 8 + (wave >> 2) * 4)) * 1024;
    const unsigned po = (lane < 48 ? (wave & 1) * 48 + lane : 96) * 8;
    u32x2 go[GS];
    unsigned pend = (1u << GS) - 1u, spins = 0;
    do {
      MEMBAR();
#pragma unroll
      for (int s = 0; s < GS; ++s)
        if (pend & (1u << s)) go[s] = LD64(rw, po, pbase + s * (8 * 1024), AUX_SC1);
#pragma unroll
      for (int s = 0; s < GS; ++s)
        if (pend & (1u << s)) { if (__all((go[s].x & go[s].y & 1u) != 0u)) pend &= ~(1u << s); }
      if (PSLEEP && pend != 0u) __builtin_amdgcn_s_sleep(PSLEEP);
    } while (pend != 0u && ++spins < SPIN_CAP);
    const int sl = lane & 3;
    const u32x2 gm = sl == 0 ? go[0] : sl == 1 ? go[1] : sl == 2 ? go[2] : go[3];
    const float ms = __uint_as_float(gm.x);
    float M = fmaxf(ms, dpp<XOR1>(ms)); M = fmaxf(M, dpp<XOR2>(M));
    const float f = __builtin_amdgcn_exp2f(ms - M);
    float Ls = f * __uint_as_float(gm.y);
    Ls += dpp<XOR1>(Ls); Ls += dpp<XOR2>(Ls);
    const float wgt = f * __builtin_amdgcn_rcpf(Ls);
    f32x2 oa = splat(rdlane(wgt, 48)) * asf2(go[0].x, go[0].y), ob = splat(rdlane(wgt, 49)) * asf2(go[1].x, go[1].y);
    oa = fma2(splat(rdlane(wgt, 50)), asf2(go[2].x, go[2].y), oa); ob = fma2(splat(rdlane(wgt, 51)), asf2(go[3].x, go[3].y), ob);
    oa += ob;
    if (lane < 48) ldu[L_O + 48 * wave + lane] = pack_bf16(oa.x, oa.y);
  }
  STAMP(4);
  {
    unsigned op[6];
#pragma unroll
    for (int i = 0; i < 6; ++i) op[i] = ldu[L_O + 48 * wave + 8 * i + r];
    float y[8];
#pragma unroll
    for (int k = 0; k < 8; ++k) y[k] = 0.f;
#pragma unroll
    for (int i = 0; i < 6; ++i) {
#pragma unroll
      for (int k = 0; k < 4; ++k) { y[k] = dot2(wop[i][0][k], op[i], y[k]); y[k + 4] = dot2(wop[i][1][k], op[i], y[k + 4]); }
    }
    const float z0 = fold32(y[0], y[4]), z1 = fold32(y[1], y[5]), z2 = fold32(y[2], y[6]), z3 = fold32(y[3], y[7]);
    const float u = fold8(fold16(z0, z2), fold16(z1, z3), bit3);
    if (c < 5) lds[L_YR + wave * 40 + fidx] = u;
  }
  __syncthreads();
  if (tid < 40) {
    const int bb = tid >= 20, t = tid - 20 * bb;
    f32x2 v[4];
#pragma unroll
    for (int w = 0; w < 4; ++w) v[w] = *reinterpret_cast<const f32x2*>(lds + L_YR + (4 * bb + w) * 40 + 2 * t);
    const f32x2 ys = (v[0] + v[1]) + (v[2] + v[3]);
    ST64((u32x2{vbit(ys.x), vbit(ys.y)}), rw, (bb * 8 * 320 + t) * 16, WS_Y + (team * 320 + cb * 20) * 16 + b * 8);
  }
  STAMP(5);
  __syncthreads();
  if (wave >= 3) {
    u32x4 gy[8];
    const unsigned yo = (tid - 192) * 16, ybase = WS_Y + b * (8 * 320 * 16);
    unsigned pend = 0xffu, spins = 0;
    do {
      MEMBAR();
#pragma unroll
      for (int s = 0; s < 8; ++s)
        if (pend & (1u << s)) gy[s] = LD128(rw, yo, ybase + s * (320 * 16), AUX_SC1);
#pragma unroll
      for (int s = 0; s < 8; ++s)
        if (pend & (1u << s)) { if (__all((gy[s].x & gy[s].y & gy[s].z & gy[s].w & 1u) != 0u)) pend &= ~(1u << s); }
      if (PSLEEP && pend != 0u) __builtin_amdgcn_s_sleep(PSLEEP);
    } while (pend != 0u && ++spins < SPIN_CAP);
    f32x2 ap[8];
#pragma unroll
    for (int s = 0; s < 8; ++s) ap[s] = asf2(gy[s].x, gy[s].y) + asf2(gy[s].z, gy[s].w);
    const f32x2 a = ((ap[0] + ap[1]) + (ap[2] + ap[3])) + ((ap[4] + ap[5]) + (ap[6] + ap[7]));
    const unsigned hp = pack_bf16(bflo(xres) + rbf(a.x), bfhi(xres) + rbf(a.y));
    ldu[L_H + tid - 192] = hp;
    float st = fold32(dot2(hp, ONES, 0.f), dot2(hp, hp, 0.f));
    st = row_sum16(fold16(st, st));
    if ((lane & 31) == 0) lds[L_ST + (wave - 3) * 2 + (lane >> 5)] = st;
  }
  __syncthreads();
  STAMP(6);
  const int d0 = 5 * (team * 16 + cb);
  float he;
  {
    const int de = d0 + (lane < 5 ? lane : 0);
    const unsigned hw = ldu[L_H + (de >> 1)];
    f32x2 sv[5];
#pragma unroll
    for (int w = 0; w < 5; ++w) sv[w] = *reinterpret_cast<const f32x2*>(lds + L_ST + 2 * w);
    const f32x2 st = (((sv[0] + sv[1]) + (sv[2] + sv[3])) + sv[4]) * splat(1.0f / D);
    he = (de & 1) ? bfhi(hw) : bflo(hw);
    const float mean1 = st.x;
    const float var1 = fmaxf(fmaf(-mean1, mean1, st.y), 0.f);
    const float i1 = ln_den_inv(var1), m1 = rbf(mean1);
    const float m2 = rbf((mean1 - m1) * i1), i2 = ln_den_inv(var1 * i1 * i1);
    const float tl = rbf(rbf(rbf(rbf(he - m1) * i1) - m2) * i2);
    constexpr float G1 = -2.0f * 0.7978845608028654f * LOG2E, G3 = G1 * 0.044715f;
    float te[5];
#pragma unroll
    for (int e = 0; e < 5; ++e) te[e] = rdlane(tl, e);
    f32x2 u[8], z[8];
#pragma unroll
    for (int e = 0; e < 5; ++e) u[e] = fma2(splat(te[e]), fw1, fb1);
    u[5] = fma2(f32x2{te[0], te[1]}, splat(gw1), splat(gb1));
    u[6] = fma2(f32x2{te[2], te[3]}, splat(gw1), splat(gb1));
    u[7] = fma2(splat(te[4]), splat(gw1), splat(gb1));
    SB();
#pragma unroll
    for (int k = 0; k < 8; ++k) z[k] = u[k] * u[k];
    SB();
#pragma unroll
    for (int k = 0; k < 8; ++k) z[k] = fma2(z[k], splat(G3), splat(G1));
    SB();
#pragma unroll
    for (int k = 0; k < 8; ++k) z[k] = z[k] * u[k];
    SB();
#pragma unroll
    for (int k = 0; k < 8; ++k) { z[k].x = __builtin_amdgcn_exp2f(z[k].x); if (k < 7) z[k].y = __builtin_amdgcn_exp2f(z[k].y); }
#pragma unroll
    for (int k = 0; k < 8; ++k) u[k] = u[k] * (k < 5 ? fw2 : splat(gw2));
    SB();
#pragma unroll
    for (int k = 0; k < 8; ++k) z[k] = z[k] + splat(1.0f);
    SB();
#pragma unroll
    for (int k = 0; k < 8; ++k) { z[k].x = __builtin_amdgcn_rcpf(z[k].x); if (k < 7) z[k].y = __builtin_amdgcn_rcpf(z[k].y); }
    SB();
#pragma unroll
    for (int k = 0; k < 8; ++k) u[k] = u[k] * z[k];
    const float ya0 = (u[0].x + u[0].y) + u[5].x, ya1 = (u[1].x + u[1].y) + u[5].y, ya2 = (u[2].x + u[2].y) + u[6].x,
                ya3 = (u[3].x + u[3].y) + u[6].y, ya4 = (u[4].x + u[4].y) + u[7].x;
    const float f4 = fold32(ya4, ya4);
    float r0 = fold8(fold16(fold32(ya0, ya1), fold32(ya2, ya3)), fold16(f4, f4), bit3);
    r0 += dpp<XOR1>(r0); r0 += dpp<XOR2>(r0); r0 += dpp<HMIR>(r0);
    const int el = bit3 ? 4 : (lane >> 5) + 2 * ((lane >> 4) & 1);
    if ((lane & 15) == 0 || lane == 8) lds[L_MR + el * 8 + wave] = r0;
  }
  __syncthreads();
  if (tid < 5) {
    typedef float f32x4 __attribute__((ext_vector_type(4)));
    const f32x4 pa = *reinterpret_cast<const f32x4*>(lds + L_MR + tid * 8), pb = *reinterpret_cast<const f32x4*>(lds + L_MR + tid * 8 + 4);
    const f32x4 ps = pa + pb;
    const float yv = (ps.x + ps.y) + (ps.z + ps.w);
    const unsigned o = pack_bf16(he + rbf(rbf(yv) + fb2), 0.f);
    __builtin_amdgcn_raw_buffer_store_b16((unsigned short)o, __builtin_amdgcn_make_buffer_rsrc((void*)out, 0, 2 * D * 2 + 4, RSRC3),
                                          tid * 2, (b * D + d0) * 2, 0);
  }
  if (tid == 0 && bid == 0)
    __builtin_amdgcn_raw_buffer_store_b64(u32x2{tlo, thi}, rw, 0, WS_EPOCH, AUX_SC1);
  STAMP(7);
}

}

extern "C" void kernel_launch(void* const* d_in, const int* in_sizes, int n_in, void* d_out, int out_size, void* d_ws,
                              size_t ws_size, hipStream_t stream) {
  (void)in_sizes; (void)n_in; (void)out_size; (void)ws_size;
  fused_kernel<<<dim3(NWG), dim3(NTHR), 0, stream>>>(
      (const char*)d_in[0], (const char*)d_in[1], (const char*)d_in[2], (const char*)d_in[3], (const char*)d_in[4],
      (const char*)d_in[6], (const char*)d_in[7], (const char*)d_in[8], (const char*)d_in[9], (char*)d_out, (char*)d_ws);
}
```

```cpp
#include <hip/hip_runtime.h>
#include <hip/hip_bf16.h>
#include <stdint.h>

namespace {

typedef unsigned u32x4 __attribute__((ext_vector_type(4)));
typedef unsigned u32x2 __attribute__((ext_vector_type(2)));
typedef float f32x2 __attribute__((ext_vector_type(2)));
typedef float f32x4 __attribute__((ext_vector_type(4)));
typedef __bf16 bf16x2 __attribute__((ext_vector_type(2)));

constexpr int D = 640, H = 32, SK = 8192, DH = 192, FF = 1536;
constexpr int NWG = 256, NTHR = 512, NWAVE = 8;
constexpr unsigned TILE_BYTES = 8 * DH * 2;
constexpr unsigned HEAD_BYTES = SK * DH * 2;
#define NOQ 0
#define NOWO 0
#define BURST 1
#define OUTAUX 0
#define PW 3
#define RA 8
#define PSLEEP 0
#define CATCH 3
#define RPRE 5
constexpr int GS = 4;
constexpr int NSW = 4, NH = NWAVE - NSW;
constexpr int STEPS = SK / 8 / (GS * NSW);
constexpr unsigned STRIDE = GS * NSW * TILE_BYTES;
constexpr int AUX_NT = 2, AUX_SC1 = 16;
constexpr unsigned RSRC3 = 0x00020000u;
constexpr unsigned OOB = 0x80000000u;

constexpr unsigned WS_EPOCH = 0;
constexpr unsigned WS_RDY = 4096;
constexpr unsigned WS_P = 8192;
constexpr unsigned WS_Y = WS_P + NWG * 1024;
constexpr unsigned WS_QP = WS_Y + 2 * 8 * 320 * 16;
constexpr unsigned WS_END = WS_QP + 32 * 2 * 8 * 96 * 16;

constexpr int L_RED = 0;
constexpr int L_ML = L_RED + NSW * DH;
constexpr int L_Q = L_ML + 16;
constexpr int L_TAG = L_Q + 96;
constexpr int L_CNT = L_TAG + 4;
constexpr int L_O = L_CNT + 8;
constexpr int L_YR = L_O + 384;
constexpr int L_H = L_YR + 320;
constexpr int L_ST = L_H + 320;
constexpr int L_MR = L_ST + 16;
constexpr int L_WO = L_MR + 64;
constexpr int L_TOTAL = 24 * 1024;
static_assert(L_WO + 24 * 2 * 64 * 4 <= L_TOTAL, "lds");
static_assert(L_WO % 4 == 0 && L_Q % 4 == 0 && L_ST % 4 == 0 && L_MR % 4 == 0, "alignment");

__device__ __forceinline__ float dot2(unsigned a, unsigned b, float c) {
  return __builtin_amdgcn_fdot2_f32_bf16(__builtin_bit_cast(bf16x2, a), __builtin_bit_cast(bf16x2, b), c, false);
}
template <int CTRL> __device__ __forceinline__ float dpp(float x) {
  return __builtin_bit_cast(float, __builtin_amdgcn_mov_dpp(__builtin_bit_cast(int, x), CTRL, 0xf, 0xf, true));
}
constexpr int XOR1 = 0xB1, XOR2 = 0x4E, HMIR = 0x141, ROR8 = 0x128;
constexpr unsigned ONES = 0x3f803f80u;
__device__ __forceinline__ float bflo(unsigned u) { return __builtin_bit_cast(float, u << 16); }
__device__ __forceinline__ float bfhi(unsigned u) { return __builtin_bit_cast(float, u & 0xffff0000u); }
__device__ __forceinline__ f32x2 bf2(unsigned u) { return f32x2{bflo(u), bfhi(u)}; }
__device__ __forceinline__ unsigned pack_bf16(float lo, float hi) {
  bf16x2 v; v[0] = (__bf16)lo; v[1] = (__bf16)hi;
  return __builtin_bit_cast(unsigned, v);
}
__device__ __forceinline__ float rbf(float x) {
  typedef float f32x2_ __attribute__((ext_vector_type(2)));
  return __builtin_bit_cast(float, __builtin_convertvector(f32x2_{0.f, x}, bf16x2));
}
__device__ __forceinline__ float fold32(float a, float b) {
  auto r = __builtin_amdgcn_permlane32_swap(__float_as_uint(a), __float_as_uint(b), false, false);
  return __uint_as_float(r[0]) + __uint_as_float(r[1]);
}
__device__ __forceinline__ float fold16(float a, float b) {
  auto r = __builtin_amdgcn_permlane16_swap(__float_as_uint(a), __float_as_uint(b), false, false);
  return __uint_as_float(r[0]) + __uint_as_float(r[1]);
}
__device__ __forceinline__ float fold8(float a, float b, bool bit3) {
  const float keep = bit3 ? b : a, send = bit3 ? a : b;
  return keep + dpp<ROR8>(send);
}
__device__ __forceinline__ float max3216(float x) {
  auto s = __builtin_amdgcn_permlane16_swap(__float_as_uint(x), __float_as_uint(x), false, false);
  x = fmaxf(__uint_as_float(s[0]), __uint_as_float(s[1]));
  auto t = __builtin_amdgcn_permlane32_swap(__float_as_uint(x), __float_as_uint(x), false, false);
  return fmaxf(__uint_as_float(t[0]), __uint_as_float(t[1]));
}
__device__ __forceinline__ float row_sum16(float x) {
  x += dpp<XOR1>(x); x += dpp<XOR2>(x); x += dpp<HMIR>(x); x += dpp<ROR8>(x);
  return x;
}
__device__ __forceinline__ unsigned long long mix64(unsigned long long z) {
  z += 0x9E3779B97F4A7C15ull;
  z = (z ^ (z >> 30)) * 0xBF58476D1CE4E5B9ull;
  z = (z ^ (z >> 27)) * 0x94D049BB133111EBull;
  return z ^ (z >> 31);
}
__device__ __forceinline__ u32x4 pack2(float a, float b, unsigned t8, unsigned thi) {
  u32x4 r; r.x = (__float_as_uint(a) >> 8) | t8; r.y = thi; r.z = (__float_as_uint(b) >> 8) | t8; r.w = thi; return r;
}
__device__ __forceinline__ unsigned tag_bad(u32x4 g, unsigned t8, unsigned thi) {
  return (((g.x ^ t8) | (g.z ^ t8)) >> 24) | (g.y ^ thi) | (g.w ^ thi);
}
__device__ __forceinline__ float g0(u32x4 g) { return __uint_as_float(g.x << 8); }
__device__ __forceinline__ float g1(u32x4 g) { return __uint_as_float(g.z << 8); }
__device__ __forceinline__ f32x2 g01(u32x4 g) { return f32x2{g0(g), g1(g)}; }
__device__ __forceinline__ unsigned vbit(float x) { return __float_as_uint(x) | 1u; }
__device__ __forceinline__ f32x2 asf2(unsigned a, unsigned b) { return f32x2{__uint_as_float(a), __uint_as_float(b)}; }
__device__ __forceinline__ float ln_den_inv(float var) {
  return __builtin_amdgcn_rcpf(rbf(__builtin_amdgcn_sqrtf(rbf(rbf(var) + 1e-5f))));
}
__device__ __forceinline__ float rdlane(float x, int l) { return __uint_as_float(__builtin_amdgcn_readlane(__float_as_uint(x), l)); }
__device__ __forceinline__ f32x2 splat(float x) { return f32x2{x, x}; }
__device__ __forceinline__ f32x2 fma2(f32x2 a, f32x2 b, f32x2 c) { return __builtin_elementwise_fma(a, b, c); }

#define SB() __builtin_amdgcn_sched_barrier(0)
#define MEMBAR() asm volatile("" ::: "memory")
#define LD128(rs, vo, so, aux) __builtin_amdgcn_raw_buffer_load_b128((rs), (vo), (so), (aux))
#define ST128(v, rs, vo, so) __builtin_amdgcn_raw_buffer_store_b128((v), (rs), (vo) + (so), 0, AUX_SC1)
#define LD64(rs, vo, so, aux) __builtin_amdgcn_raw_buffer_load_b64((rs), (vo), (so), (aux))
#define ST64(v, rs, vo, so) __builtin_amdgcn_raw_buffer_store_b64((v), (rs), (vo), (so), AUX_SC1)
#define WAIT_VM3() __builtin_amdgcn_s_waitcnt(0x0F73)
#define STAMP(i) do { } while (0)

constexpr unsigned SPIN_CAP = 1u << 17;

__global__ void __launch_bounds__(NTHR, 2)
fused_kernel(const char* x, const char* kc, const char* vc, const char* wq, const char* wo, const char* w1,
             const char* b1, const char* w2, const char* b2, char* out, char* ws) {
  __shared__ __attribute__((aligned(16))) float lds[L_TOTAL];
  unsigned* const ldu = reinterpret_cast<unsigned*>(lds);
  const int tid = threadIdx.x, lane = tid & 63, wave = __builtin_amdgcn_readfirstlane(tid >> 6);
  const int bid = blockIdx.x, team = bid & 7, jj = bid >> 3, hl = jj >> 3, b = (jj >> 2) & 1, sp = jj & 3;
  const int h = team * 4 + hl, cb = hl * 4 + sp, pq = jj & 7;
  const int r = lane >> 3, c = lane & 7;
  const bool bit3 = (lane & 8) != 0;
  const int fidx = 8 * c + 4 * (lane >> 5) + 2 * ((lane >> 4) & 1) + (bit3 ? 1 : 0);
  STAMP(0);
  asm volatile("" : "+s"(x), "+s"(kc), "+s"(vc), "+s"(wq), "+s"(wo), "+s"(w1), "+s"(b1), "+s"(w2), "+s"(b2), "+s"(out), "+s"(ws));
  const __amdgpu_buffer_rsrc_t rw = __builtin_amdgcn_make_buffer_rsrc((void*)ws, 0, WS_END, RSRC3);
  const __amdgpu_buffer_rsrc_t rx = __builtin_amdgcn_make_buffer_rsrc((void*)x, 0, 2 * D * 2, RSRC3);
  constexpr float SCALE = 0.07216878364870322f, LOG2E = 1.4426950408889634f;
  unsigned vzero;
  asm volatile("v_mov_b32 %0, 0" : "=v"(vzero));

  f32x2 fw1, fb1, fw2; float gw1, gb1, gw2, fb2;
  unsigned xres;
  unsigned short a1[3], a2[3], a3[3], a4;
#define LOAD_SMALL() { \
    const __amdgpu_buffer_rsrc_t r1 = __builtin_amdgcn_make_buffer_rsrc((void*)w1, 0, FF * 2 + 4, RSRC3); \
    const __amdgpu_buffer_rsrc_t r2 = __builtin_amdgcn_make_buffer_rsrc((void*)b1, 0, FF * 2 + 4, RSRC3); \
    const __amdgpu_buffer_rsrc_t r3 = __builtin_amdgcn_make_buffer_rsrc((void*)w2, 0, FF * 2 + 4, RSRC3); \
    _Pragma("unroll") for (int k = 0; k < 3; ++k) { \
      a1[k] = __builtin_amdgcn_raw_buffer_load_b16(r1, tid * 2 + 1024 * k, 0, 0); \
      a2[k] = __builtin_amdgcn_raw_buffer_load_b16(r2, tid * 2 + 1024 * k, 0, 0); \
      a3[k] = __builtin_amdgcn_raw_buffer_load_b16(r3, tid * 2 + 1024 * k, 0, 0); \
    } \
    a4 = __builtin_amdgcn_raw_buffer_load_b16(__builtin_amdgcn_make_buffer_rsrc((void*)b2, 0, 4, RSRC3), vzero, 0, 0); \
    xres = __builtin_amdgcn_raw_buffer_load_b32(rx, tid >= 192 ? (tid - 192) * 4 : OOB, b * D * 2, 0); \
    MEMBAR(); }
#define CONV_SMALL() { \
    fw1 = f32x2{bflo(a1[0]), bflo(a1[1])}; fb1 = f32x2{bflo(a2[0]), bflo(a2[1])}; fw2 = f32x2{bflo(a3[0]), bflo(a3[1])}; \
    gw1 = bflo(a1[2]); gb1 = bflo(a2[2]); gw2 = bflo(a3[2]); fb2 = bflo(a4); \
    asm volatile("" : "+v"(fw1), "+v"(fb1), "+v"(fw2), "+v"(gw1), "+v"(gb1), "+v"(gw2), "+v"(fb2), "+v"(xres) :: "memory"); }

  u32x4 wop[6][2];
  unsigned* const wl = ldu + L_WO + ((wave & 3) * 12 * 64 + lane) * 4;
  if (wave >= NH) {
    const int sw = wave - NH;
    const size_t kvbase = (size_t)(b * H + h) * HEAD_BYTES;
    const __amdgpu_buffer_rsrc_t rk = __builtin_amdgcn_make_buffer_rsrc((void*)(kc + kvbase), 0, HEAD_BYTES, RSRC3);
    const __amdgpu_buffer_rsrc_t rv = __builtin_amdgcn_make_buffer_rsrc((void*)(vc + kvbase), 0, HEAD_BYTES, RSRC3);
    const unsigned kvo = r * (DH * 2) + c * 16;
    u32x4 kbuf[RPRE][3], vbuf[RPRE][3];
    unsigned so = (sp * NSW + sw) * TILE_BYTES + (h & 1) * RA * STRIDE;
    static_assert(RA + RPRE + 2 < STEPS, "no wrap before the main loop");
#pragma unroll
    for (int j = 0; j < RPRE; ++j) {
      if (j >= BURST) { __builtin_amdgcn_s_waitcnt(0x0F70 | PW); SB(); }
#pragma unroll
      for (int t = 0; t < 3; ++t) { kbuf[j][t] = LD128(rk, kvo + t * 128, so, AUX_NT); SB(); }
      if (j >= BURST) { __builtin_amdgcn_s_waitcnt(0x0F70 | PW); SB(); }
#pragma unroll
      for (int t = 0; t < 3; ++t) { vbuf[j][t] = LD128(rv, kvo + t * 128, so, AUX_NT); SB(); }
      so += STRIDE;
    }
    LOAD_SMALL();
    STAMP(8);
    __syncthreads();
    u32x4 q[3];
#pragma unroll
    for (int t = 0; t < 3; ++t) q[t] = *reinterpret_cast<const u32x4*>(ldu + L_Q + (8 * t + c) * 4);
    STAMP(1);
    float m = -INFINITY, l = 0.f;
    f32x2 acc[12];
#pragma unroll
    for (int k = 0; k < 12; ++k) acc[k] = f32x2{0.f, 0.f};

#define PIN12(A) asm volatile("" : "+v"(A[0]), "+v"(A[1]), "+v"(A[2]), "+v"(A[3]), "+v"(A[4]), "+v"(A[5]), "+v"(A[6]), "+v"(A[7]), \
    "+v"(A[8]), "+v"(A[9]), "+v"(A[10]), "+v"(A[11]) :: "memory")
#define STEP(KB, VB, REFILL) { \
    float s0_ = 0.f, s1_ = 0.f, s2_ = 0.f; \
    _Pragma("unroll") for (int j_ = 0; j_ < 4; ++j_) { s0_ = dot2(KB[0][j_], q[0][j_], s0_); s1_ = dot2(KB[1][j_], q[1][j_], s1_); s2_ = dot2(KB[2][j_], q[2][j_], s2_); } \
    asm volatile("" : "+v"(s0_), "+v"(s1_), "+v"(s2_) :: "memory"); \
    SB(); \
    if (REFILL) { _Pragma("unroll") for (int u_ = 0; u_ < 3; ++u_) KB[u_] = LD128(rk, kvo + u_ * 128, so, AUX_NT); } \
    SB(); \
    float s_ = s0_ + s1_ + s2_; \
    s_ += dpp<XOR1>(s_); s_ += dpp<XOR2>(s_); s_ += dpp<HMIR>(s_); \
    s_ = rbf(rbf(s_) * SCALE) * LOG2E; \
    if (__any(s_ > m)) { \
      const float mn_ = fmaxf(m, max3216(fmaxf(s_, dpp<ROR8>(s_)))); \
      const float al_ = __builtin_amdgcn_exp2f(m - mn_); \
      m = mn_; l *= al_; \
      _Pragma("unroll") for (int k_ = 0; k_ < 12; ++k_) acc[k_] *= splat(al_); \
    } \
    float p_ = __builtin_amdgcn_exp2f(s_ - m); \
    asm volatile("" : "+v"(p_)); \
    SB(); \
    f32x2 vf_[12]; \
    _Pragma("unroll") for (int t_ = 0; t_ < 3; ++t_) \
      _Pragma("unroll") for (int j_ = 0; j_ < 4; ++j_) vf_[t_ * 4 + j_] = bf2(VB[t_][j_]); \
    PIN12(vf_); \
    SB(); \
    if (REFILL) { _Pragma("unroll") for (int u_ = 0; u_ < 3; ++u_) VB[u_] = LD128(rv, kvo + u_ * 128, so, AUX_NT); } \
    SB(); \
    l += p_; \
    _Pragma("unroll") for (int k_ = 0; k_ < 12; ++k_) acc[k_] = fma2(splat(p_), vf_[k_], acc[k_]); \
    PIN12(acc); \
    SB(); }

#define STEP2(KB, VB, KN, VN) { \
    __builtin_amdgcn_s_waitcnt(0x0F73); \
    SB(); \
    _Pragma("unroll") for (int u_ = 0; u_ < 3; ++u_) KN[u_] = LD128(rk, kvo + u_ * 128, so, AUX_NT); \
    MEMBAR(); \
    SB(); \
    float s0_ = 0.f, s1_ = 0.f, s2_ = 0.f; \
    _Pragma("unroll") for (int j_ = 0; j_ < 4; ++j_) { s0_ = dot2(KB[0][j_], q[0][j_], s0_); s1_ = dot2(KB[1][j_], q[1][j_], s1_); s2_ = dot2(KB[2][j_], q[2][j_], s2_); } \
    float s_ = s0_ + s1_ + s2_; \
    s_ += dpp<XOR1>(s_); s_ += dpp<XOR2>(s_); s_ += dpp<HMIR>(s_); \
    s_ = rbf(rbf(s_) * SCALE) * LOG2E; \
    if (__any(s_ > m)) { \
      const float mn_ = fmaxf(m, max3216(fmaxf(s_, dpp<ROR8>(s_)))); \
      const float al_ = __builtin_amdgcn_exp2f(m - mn_); \
      m = mn_; l *= al_; \
      _Pragma("unroll") for (int k_ = 0; k_ < 12; ++k_) acc[k_] *= splat(al_); \
    } \
    float p_ = __builtin_amdgcn_exp2f(s_ - m); \
    asm volatile("" : "+v"(p_) :: "memory"); \
    SB(); \
    __builtin_amdgcn_s_waitcnt(0x0F73); \
    SB(); \
    _Pragma("unroll") for (int u_ = 0; u_ < 3; ++u_) VN[u_] = LD128(rv, kvo + u_ * 128, so, AUX_NT); \
    MEMBAR(); \
    SB(); \
    l += p_; \
    _Pragma("unroll") for (int t_ = 0; t_ < 3; ++t_) \
      _Pragma("unroll") for (int j_ = 0; j_ < 4; ++j_) acc[t_ * 4 + j_] = fma2(splat(p_), bf2(VB[t_][j_]), acc[t_ * 4 + j_]); \
    PIN12(acc); \
    SB(); }

    static_assert(RPRE % 2 == 1 && CATCH >= 1 && CATCH < RPRE, "parity of the main loop");
    STEP(kbuf[0], vbuf[0], true);
#pragma unroll
    for (int jb = 1; jb <= CATCH; ++jb) STEP(kbuf[jb], vbuf[jb], false);
    so += STRIDE;
    WAIT_VM3(); SB();
#pragma unroll
    for (int t = 0; t < 3; ++t) kbuf[1][t] = LD128(rk, kvo + t * 128, so, AUX_NT);
    SB(); WAIT_VM3(); SB();
#pragma unroll
    for (int t = 0; t < 3; ++t) vbuf[1][t] = LD128(rv, kvo + t * 128, so, AUX_NT);
    MEMBAR(); SB();
#pragma unroll
    for (int jb = CATCH + 1; jb < RPRE; ++jb) STEP(kbuf[jb], vbuf[jb], false);
    STEP(kbuf[0], vbuf[0], false);
#pragma nounroll
    for (int i = RPRE + 1; i < STEPS - 2; i += 2) {
      so += STRIDE;
      if (so >= HEAD_BYTES) so -= HEAD_BYTES;
      STEP2(kbuf[1], vbuf[1], kbuf[0], vbuf[0]);
      so += STRIDE;
      if (so >= HEAD_BYTES) so -= HEAD_BYTES;
      STEP2(kbuf[0], vbuf[0], kbuf[1], vbuf[1]);
    }
    so += STRIDE;
    if (so >= HEAD_BYTES) so -= HEAD_BYTES;
    STEP2(kbuf[1], vbuf[1], kbuf[0], vbuf[0]);
    STEP(kbuf[0], vbuf[0], false);
    STAMP(2);
    {
      float lw = l + dpp<ROR8>(l);
      lw = fold16(lw, lw); lw = fold32(lw, lw);
      float* const red = lds + L_RED + sw * DH + fidx;
#pragma unroll
      for (int t = 0; t < 3; ++t) {
        const float z0 = fold32(acc[t * 4 + 0].x, acc[t * 4 + 2].x), z1 = fold32(acc[t * 4 + 0].y, acc[t * 4 + 2].y);
        const float z2 = fold32(acc[t * 4 + 1].x, acc[t * 4 + 3].x), z3 = fold32(acc[t * 4 + 1].y, acc[t * 4 + 3].y);
        red[64 * t] = fold8(fold16(z0, z2), fold16(z1, z3), bit3);
      }
      unsigned arrived = 0;
      if (lane == 0) {
        *reinterpret_cast<f32x2*>(lds + L_ML + 2 * sw) = f32x2{m, lw};
        arrived = __hip_atomic_fetch_add(ldu + L_CNT, 1u, __ATOMIC_ACQ_REL, __HIP_MEMORY_SCOPE_WORKGROUP);
      }
      if (__builtin_amdgcn_readfirstlane(arrived) == NSW - 1) {
        const int d4 = lane < 48 ? 4 * lane : 0;
        const f32x4 ml01 = *reinterpret_cast<const f32x4*>(lds + L_ML), ml23 = *reinterpret_cast<const f32x4*>(lds + L_ML + 4);
        f32x4 v[NSW];
#pragma unroll
        for (int w = 0; w < NSW; ++w) v[w] = *reinterpret_cast<const f32x4*>(lds + L_RED + w * DH + d4);
        const float M = __uint_as_float(vbit(fmaxf(fmaxf(ml01.x, ml01.z), fmaxf(ml23.x, ml23.z))));
        const float f0 = __builtin_amdgcn_exp2f(ml01.x - M), f1 = __builtin_amdgcn_exp2f(ml01.z - M);
        const float f2 = __builtin_amdgcn_exp2f(ml23.x - M), f3 = __builtin_amdgcn_exp2f(ml23.z - M);
        f32x4 o = (v[0] * f0 + v[1] * f1) + (v[2] * f2 + v[3] * f3);
        const float Ls = (ml01.y * f0 + ml01.w * f1) + (ml23.y * f2 + ml23.w * f3);
        if (lane >= 48) { o.x = M; o.y = Ls; }
        ST128((u32x4{vbit(o.x), vbit(o.y), vbit(o.z), vbit(o.w)}), rw, lane <= 48 ? lane * 16 : OOB, WS_P + bid * 1024);
      }
    }
    STAMP(3);
  } else {
    if (wave < 3 && !NOQ) {
      const __amdgpu_buffer_rsrc_t rq = __builtin_amdgcn_make_buffer_rsrc((void*)wq, 0, D * H * DH * 2, RSRC3);
      unsigned xs[2][5], xp[2];
#pragma unroll
      for (int bb = 0; bb < 2; ++bb) {
#pragma unroll
        for (int i = 0; i < 5; ++i) xs[bb][i] = __builtin_amdgcn_raw_buffer_load_b32(rx, lane * 4 + i * 256 + bb * D * 2, 0, 0);
        xp[bb] = __builtin_amdgcn_raw_buffer_load_b32(rx, (c < 5 ? (8 * c + r) * 4 : 0) + bb * D * 2, pq * 160, 0);
      }
      const u32x2 ep = __builtin_amdgcn_raw_buffer_load_b64(rw, vzero, WS_EPOCH, AUX_SC1);
      SB();
      u32x4 wqr[5][2];
      {
        const unsigned vo = (unsigned)(2 * r) * (H * DH * 2) + c * 16;
        const unsigned qso = (unsigned)(80 * pq) * (H * DH * 2) + h * (DH * 2) + wave * 128;
#pragma unroll
        for (int i = 0; i < 5; ++i)
#pragma unroll
          for (int e = 0; e < 2; ++e) { wqr[i][e] = LD128(rq, vo, qso + (16 * i + e) * (H * DH * 2), AUX_NT); SB(); }
      }
      float s0 = 0.f, s1 = 0.f, q0 = 0.f, q1 = 0.f;
#pragma unroll
      for (int i = 0; i < 5; ++i) {
        s0 = dot2(xs[0][i], ONES, s0); q0 = dot2(xs[0][i], xs[0][i], q0);
        s1 = dot2(xs[1][i], ONES, s1); q1 = dot2(xs[1][i], xs[1][i], q1);
      }
      const float st = row_sum16(fold16(fold32(s0, s1), fold32(q0, q1)));
      auto sw2 = __builtin_amdgcn_permlane16_swap(__float_as_uint(st), __float_as_uint(st), false, false);
      const float mean = __uint_as_float(sw2[0]) * (1.0f / D);
      const float var = fmaxf(fmaf(-mean, mean, __uint_as_float(sw2[1]) * (1.0f / D)), 0.f);
      const float inv = ln_den_inv(var), mb = rbf(mean);
      const float m0 = rdlane(mb, 0), m1 = rdlane(mb, 32), i0 = rdlane(inv, 0), i1 = rdlane(inv, 32);
      const unsigned xn0 = pack_bf16(rbf(bflo(xp[0]) - m0) * i0, rbf(bfhi(xp[0]) - m0) * i0);
      const unsigned xn1 = pack_bf16(rbf(bflo(xp[1]) - m1) * i1, rbf(bfhi(xp[1]) - m1) * i1);
      const unsigned long long tg = mix64(((unsigned long long)(unsigned)__builtin_amdgcn_readfirstlane(ep.y) << 32) | (unsigned)__builtin_amdgcn_readfirstlane(ep.x));
      const unsigned thi = (unsigned)(tg >> 32), t8 = (unsigned)tg & 0xff000000u;
      float a0[8], a1q[8];
#pragma unroll
      for (int k = 0; k < 8; ++k) { a0[k] = 0.f; a1q[k] = 0.f; }
#define QGROUP(I) { \
        unsigned b0_ = __builtin_amdgcn_update_dpp(0u, xn0, 0x150 + I, 0xf, 0x3, false); b0_ = __builtin_amdgcn_update_dpp(b0_, xn0, 0x158 + I, 0xf, 0xc, false); \
        unsigned b1_ = __builtin_amdgcn_update_dpp(0u, xn1, 0x150 + I, 0xf, 0x3, false); b1_ = __builtin_amdgcn_update_dpp(b1_, xn1, 0x158 + I, 0xf, 0xc, false); \
        _Pragma("unroll") for (int j = 0; j < 4; ++j) { \
          const unsigned plo = __builtin_amdgcn_perm(wqr[I][1][j], wqr[I][0][j], 0x05040100u), phi = __builtin_amdgcn_perm(wqr[I][1][j], wqr[I][0][j], 0x07060302u); \
          a0[2 * j] = dot2(plo, b0_, a0[2 * j]); a0[2 * j + 1] = dot2(phi, b0_, a0[2 * j + 1]); \
          a1q[2 * j] = dot2(plo, b1_, a1q[2 * j]); a1q[2 * j + 1] = dot2(phi, b1_, a1q[2 * j + 1]); } }
      QGROUP(0) QGROUP(1) QGROUP(2) QGROUP(3) QGROUP(4)
      float z[8];
#pragma unroll
      for (int k = 0; k < 8; ++k) z[k] = fold32(a0[k], a1q[k]);
      float y[4];
#pragma unroll
      for (int k = 0; k < 4; ++k) y[k] = fold16(z[k], z[k + 4]);
      const float u0 = fold8(y[0], y[2], bit3), u1 = fold8(y[1], y[3], bit3);
      const unsigned pidx = 32 * wave + 4 * c + 2 * ((lane >> 4) & 1) + (bit3 ? 1 : 0);
      ST128(pack2(u0, u1, t8, thi), rw, ((lane >> 5) * 8 * 96 + pidx) * 16, WS_QP + (h * 16 + pq) * (96 * 16));
      STAMP(8);
      asm volatile("s_waitcnt vmcnt(0)" ::: "memory");
      {
        const unsigned qo = ((lane >> 5) * 96 + 32 * wave + (lane & 31)) * 16;
        const unsigned qb = WS_QP + (h * 16 + b * 8) * (96 * 16);
        u32x4 gq[4];
        for (unsigned spins = 0;; ++spins) {
          MEMBAR();
#pragma unroll
          for (int k = 0; k < 4; ++k) gq[k] = LD128(rw, qo, qb + k * (2 * 96 * 16), AUX_SC1);
          unsigned bad = 0;
#pragma unroll
          for (int k = 0; k < 4; ++k) bad |= tag_bad(gq[k], t8, thi);
          if (__all(bad == 0u) || spins > SPIN_CAP) break;
        }
        const f32x2 qq = (g01(gq[0]) + g01(gq[1])) + (g01(gq[2]) + g01(gq[3]));
        const float qa = fold32(qq.x, qq.x), qc = fold32(qq.y, qq.y);
        if (lane < 32) ldu[L_Q + 32 * wave + lane] = pack_bf16(qa, qc);
      }
      if (tid == 0) *reinterpret_cast<u32x2*>(ldu + L_TAG) = u32x2{(unsigned)tg, thi};
    }
    const unsigned yoff = ((lane >= 20 ? 8 * 320 : 0) + (lane >= 20 ? lane - 20 : lane)) * 16;
    const unsigned ybo = WS_Y + (team * 320 + cb * 20) * 16 + b * 8;
    if (wave == 3) {
      if (lane == 0) ldu[L_CNT] = 0u;
      const u32x2 ep = LD64(rw, vzero, WS_EPOCH, AUX_SC1);
      ST128((u32x4{0u, 0u, 0u, 0u}), rw, lane * 16, WS_P + bid * 1024);
      ST64((u32x2{0u, 0u}), rw, lane < 40 ? yoff : OOB, ybo);
      asm volatile("s_waitcnt vmcnt(0)" ::: "memory");
      const unsigned long long tg = mix64(((unsigned long long)(unsigned)__builtin_amdgcn_readfirstlane(ep.y) << 32) | (unsigned)__builtin_amdgcn_readfirstlane(ep.x));
      ST64((u32x2{(unsigned)tg, (unsigned)(tg >> 32)}), rw, lane == 0 ? 0u : OOB, WS_RDY + (team * 32 + jj) * 8);
    }
    if (NOQ && wave == 0) {
      const u32x2 ep = __builtin_amdgcn_raw_buffer_load_b64(rw, vzero, WS_EPOCH, AUX_SC1);
      const unsigned long long tg = mix64(((unsigned long long)(unsigned)__builtin_amdgcn_readfirstlane(ep.y) << 32) | (unsigned)__builtin_amdgcn_readfirstlane(ep.x));
      if (tid == 0) *reinterpret_cast<u32x2*>(ldu + L_TAG) = u32x2{(unsigned)tg, (unsigned)(tg >> 32)};
      ldu[L_Q + lane] = 0x3c003c00; ldu[L_Q + 32 + lane] = 0x3c003c00;
    }
    STAMP(9);
    __syncthreads();
    STAMP(1);
    LOAD_SMALL();
    const __amdgpu_buffer_rsrc_t ro = __builtin_amdgcn_make_buffer_rsrc((void*)wo, 0, D * H * DH * 2, RSRC3);
    const unsigned vo = c < 5 ? (unsigned)(2 * r) * (D * 2) + c * 16 : OOB;
    const unsigned wso = NOWO ? OOB : (unsigned)(768 * team + 384 * b + 96 * wave) * (D * 2) + cb * 80;
#pragma unroll
    for (int i = 0; i < 6; ++i) {
      const u32x4 e0 = LD128(ro, vo, wso + 16 * i * (D * 2), AUX_NT);
      const u32x4 e1 = LD128(ro, vo + D * 2, wso + 16 * i * (D * 2), AUX_NT);
      u32x4 p0, p1;
      p0.x = __builtin_amdgcn_perm(e1.x, e0.x, 0x05040100u); p0.y = __builtin_amdgcn_perm(e1.x, e0.x, 0x07060302u);
      p0.z = __builtin_amdgcn_perm(e1.y, e0.y, 0x05040100u); p0.w = __builtin_amdgcn_perm(e1.y, e0.y, 0x07060302u);
      p1.x = __builtin_amdgcn_perm(e1.z, e0.z, 0x05040100u); p1.y = __builtin_amdgcn_perm(e1.z, e0.z, 0x07060302u);
      p1.z = __builtin_amdgcn_perm(e1.w, e0.w, 0x05040100u); p1.w = __builtin_amdgcn_perm(e1.w, e0.w, 0x07060302u);
      wop[i][0] = p0; wop[i][1] = p1;
      *reinterpret_cast<u32x4*>(wl + (2 * i) * 256) = p0;
      *reinterpret_cast<u32x4*>(wl + (2 * i + 1) * 256) = p1;
      MEMBAR();
      SB();
    }
    if (wave == 0) {
      const u32x2 tg = *reinterpret_cast<const u32x2*>(ldu + L_TAG);
      for (unsigned spins = 0; spins < SPIN_CAP; ++spins) {
        MEMBAR();
        const u32x4 r0 = LD128(rw, lane * 16, WS_RDY, AUX_SC1), r1 = LD128(rw, lane * 16 + 1024, WS_RDY, AUX_SC1);
        const unsigned bad = (r0.x ^ tg.x) | (r0.y ^ tg.y) | (r0.z ^ tg.x) | (r0.w ^ tg.y) | (r1.x ^ tg.x) | (r1.y ^ tg.y) | (r1.z ^ tg.x) | (r1.w ^ tg.y);
        if (__all(bad == 0u)) break;
        __builtin_amdgcn_s_sleep(32);
      }
    }
  }
  const u32x2 tg2 = *reinterpret_cast<const u32x2*>(ldu + L_TAG);
  const unsigned tlo = __builtin_amdgcn_readfirstlane(tg2.x), thi = __builtin_amdgcn_readfirstlane(tg2.y);

  __syncthreads();
  {
    const unsigned pbase = WS_P + (unsigned)(team + 8 * ((2 * b + ((wave >> 1) & 1)) * 8 + (wave >> 2) * 4)) * 1024;
    const unsigned po = (lane < 48 ? (wave & 1) * 48 + lane : 96) * 8;
    u32x2 go[GS];
    MEMBAR();
#pragma unroll
    for (int s = 0; s < GS; ++s) go[s] = LD64(rw, po, pbase + s * (8 * 1024), AUX_SC1);
    MEMBAR();
    SB();
    if (wave >= NH) {
#pragma unroll
      for (int i = 0; i < 6; ++i) {
        wop[i][0] = *reinterpret_cast<const u32x4*>(wl + (2 * i) * 256);
        wop[i][1] = *reinterpret_cast<const u32x4*>(wl + (2 * i + 1) * 256);
      }
    }
    MEMBAR();
    unsigned pend = 0, spins = 0;
#pragma unroll
    for (int s = 0; s < GS; ++s)
      if (!__all((go[s].x & go[s].y & 1u) != 0u)) pend |= 1u << s;
    while (pend != 0u && ++spins < SPIN_CAP) {
      MEMBAR();
#pragma unroll
      for (int s = 0; s < GS; ++s)
        if (pend & (1u << s)) go[s] = LD64(rw, po, pbase + s * (8 * 1024), AUX_SC1);
#pragma unroll
      for (int s = 0; s < GS; ++s)
        if (pend & (1u << s)) { if (__all((go[s].x & go[s].y & 1u) != 0u)) pend &= ~(1u << s); }
    }
    const int sl = lane & 3;
    const u32x2 gm = sl == 0 ? go[0] : sl == 1 ? go[1] : sl == 2 ? go[2] : go[3];
    const float ms = __uint_as_float(gm.x);
    float M = fmaxf(ms, dpp<XOR1>(ms)); M = fmaxf(M, dpp<XOR2>(M));
    const float f = __builtin_amdgcn_exp2f(ms - M);
    float Ls = f * __uint_as_float(gm.y);
    Ls += dpp<XOR1>(Ls); Ls += dpp<XOR2>(Ls);
    const float wgt = f * __builtin_amdgcn_rcpf(Ls);
    f32x2 oa = splat(rdlane(wgt, 48)) * asf2(go[0].x, go[0].y), ob = splat(rdlane(wgt, 49)) * asf2(go[1].x, go[1].y);
    oa = fma2(splat(rdlane(wgt, 50)), asf2(go[2].x, go[2].y), oa); ob = fma2(splat(rdlane(wgt, 51)), asf2(go[3].x, go[3].y), ob);
    oa += ob;
    if (lane < 48) ldu[L_O + 48 * wave + lane] = pack_bf16(oa.x, oa.y);
  }
  STAMP(4);
  {
    unsigned op[6];
#pragma unroll
    for (int i = 0; i < 6; ++i) op[i] = ldu[L_O + 48 * wave + 8 * i + r];
    float y[8];
#pragma unroll
    for (int k = 0; k < 8; ++k) y[k] = 0.f;
#pragma unroll
    for (int i = 0; i < 6; ++i) {
#pragma unroll
      for (int k = 0; k < 4; ++k) { y[k] = dot2(wop[i][0][k], op[i], y[k]); y[k + 4] = dot2(wop[i][1][k], op[i], y[k + 4]); }
    }
    const float z0 = fold32(y[0], y[4]), z1 = fold32(y[1], y[5]), z2 = fold32(y[2], y[6]), z3 = fold32(y[3], y[7]);
    const float u = fold8(fold16(z0, z2), fold16(z1, z3), bit3);
    if (c < 5) lds[L_YR + wave * 40 + fidx] = u;
  }
  __syncthreads();
  if (tid < 40) {
    const int bb = tid >= 20, t = tid - 20 * bb;
    f32x2 v[4];
#pragma unroll
    for (int w = 0; w < 4; ++w) v[w] = *reinterpret_cast<const f32x2*>(lds + L_YR + (4 * bb + w) * 40 + 2 * t);
    const f32x2 ys = (v[0] + v[1]) + (v[2] + v[3]);
    ST64((u32x2{vbit(ys.x), vbit(ys.y)}), rw, (bb * 8 * 320 + t) * 16, WS_Y + (team * 320 + cb * 20) * 16 + b * 8);
  }
  STAMP(5);
  __syncthreads();
  CONV_SMALL();
  if (wave >= 3) {
    u32x4 gy[8];
    const unsigned yo = (tid - 192) * 16, ybase = WS_Y + b * (8 * 320 * 16);
    unsigned pend = 0xffu, spins = 0;
    do {
      MEMBAR();
#pragma unroll
      for (int s = 0; s < 8; ++s)
        if (pend & (1u << s)) gy[s] = LD128(rw, yo, ybase + s * (320 * 16), AUX_SC1);
#pragma unroll
      for (int s = 0; s < 8; ++s)
        if (pend & (1u << s)) { if (__all((gy[s].x & gy[s].y & gy[s].z & gy[s].w & 1u) != 0u)) pend &= ~(1u << s); }
      if (PSLEEP && pend != 0u) __builtin_amdgcn_s_sleep(PSLEEP);
    } while (pend != 0u && ++spins < SPIN_CAP);
    f32x2 ap[8];
#pragma unroll
    for (int s = 0; s < 8; ++s) ap[s] = asf2(gy[s].x, gy[s].y) + asf2(gy[s].z, gy[s].w);
    const f32x2 a = ((ap[0] + ap[1]) + (ap[2] + ap[3])) + ((ap[4] + ap[5]) + (ap[6] + ap[7]));
    const unsigned hp = pack_bf16(bflo(xres) + rbf(a.x), bfhi(xres) + rbf(a.y));
    ldu[L_H + tid - 192] = hp;
    float st = fold32(dot2(hp, ONES, 0.f), dot2(hp, hp, 0.f));
    st = row_sum16(fold16(st, st));
    if ((lane & 31) == 0) lds[L_ST + (wave - 3) * 2 + (lane >> 5)] = st;
  }
  __syncthreads();
  STAMP(6);
  const int d0 = 5 * (team * 16 + cb);
  float he;
  {
    const int de = d0 + (lane < 5 ? lane : 0);
    const unsigned hw = ldu[L_H + (de >> 1)];
    f32x2 sv[5];
#pragma unroll
    for (int w = 0; w < 5; ++w) sv[w] = *reinterpret_cast<const f32x2*>(lds + L_ST + 2 * w);
    const f32x2 st = (((sv[0] + sv[1]) + (sv[2] + sv[3])) + sv[4]) * splat(1.0f / D);
    he = (de & 1) ? bfhi(hw) : bflo(hw);
    const float mean1 = st.x;
    const float var1 = fmaxf(fmaf(-mean1, mean1, st.y), 0.f);
    const float i1 = ln_den_inv(var1), m1 = rbf(mean1);
    const float m2 = rbf((mean1 - m1) * i1), i2 = ln_den_inv(var1 * i1 * i1);
    const float tl = rbf(rbf(rbf(rbf(he - m1) * i1) - m2) * i2);
    constexpr float G1 = -2.0f * 0.7978845608028654f * LOG2E, G3 = G1 * 0.044715f;
    float te[5];
#pragma unroll
    for (int e = 0; e < 5; ++e) te[e] = rdlane(tl, e);
    f32x2 u[8], z[8];
#pragma unroll
    for (int e = 0; e < 5; ++e) u[e] = fma2(splat(te[e]), fw1, fb1);
    u[5] = fma2(f32x2{te[0], te[1]}, splat(gw1), splat(gb1));
    u[6] = fma2(f32x2{te[2], te[3]}, splat(gw1), splat(gb1));
    u[7] = fma2(splat(te[4]), splat(gw1), splat(gb1));
    SB();
#pragma unroll
    for (int k = 0; k < 8; ++k) z[k] = u[k] * u[k];
    SB();
#pragma unroll
    for (int k = 0; k < 8; ++k) z[k] = fma2(z[k], splat(G3), splat(G1));
    SB();
#pragma unroll
    for (int k = 0; k < 8; ++k) z[k] = z[k] * u[k];
    SB();
#pragma unroll
    for (int k = 0; k < 8; ++k) { z[k].x = __builtin_amdgcn_exp2f(z[k].x); if (k < 7) z[k].y = __builtin_amdgcn_exp2f(z[k].y); }
#pragma unroll
    for (int k = 0; k < 8; ++k) u[k] = u[k] * (k < 5 ? fw2 : splat(gw2));
    SB();
#pragma unroll
    for (int k = 0; k < 8; ++k) z[k] = z[k] + splat(1.0f);
    SB();
#pragma unroll
    for (int k = 0; k < 8; ++k) { z[k].x = __builtin_amdgcn_rcpf(z[k].x); if (k < 7) z[k].y = __builtin_amdgcn_rcpf(z[k].y); }
    SB();
#pragma unroll
    for (int k = 0; k < 8; ++k) u[k] = u[k] * z[k];
    const float ya0 = (u[0].x + u[0].y) + u[5].x, ya1 = (u[1].x + u[1].y) + u[5].y, ya2 = (u[2].x + u[2].y) + u[6].x,
                ya3 = (u[3].x + u[3].y) + u[6].y, ya4 = (u[4].x + u[4].y) + u[7].x;
    const float f4 = fold32(ya4, ya4);
    float r0 = fold8(fold16(fold32(ya0, ya1), fold32(ya2, ya3)), fold16(f4, f4), bit3);
    r0 += dpp<XOR1>(r0); r0 += dpp<XOR2>(r0); r0 += dpp<HMIR>(r0);
    const int el = bit3 ? 4 : (lane >> 5) + 2 * ((lane >> 4) & 1);
    if ((lane & 15) == 0 || lane == 8) lds[L_MR + el * 8 + wave] = r0;
  }
  __syncthreads();
  if (tid < 5) {
    const f32x4 pa = *reinterpret_cast<const f32x4*>(lds + L_MR + tid * 8), pb = *reinterpret_cast<const f32x4*>(lds + L_MR + tid * 8 + 4);
    const f32x4 ps = pa + pb;
    const float yv = (ps.x + ps.y) + (ps.z + ps.w);
    const unsigned o = pack_bf16(he + rbf(rbf(yv) + fb2), 0.f);
    __builtin_amdgcn_raw_buffer_store_b16((unsigned short)o, __builtin_amdgcn_make_buffer_rsrc((void*)out, 0, 2 * D * 2 + 4, RSRC3),
                                          tid * 2, (b * D + d0) * 2, OUTAUX);
  }
  if (tid == 0 && bid == 0)
    __builtin_amdgcn_raw_buffer_store_b64(u32x2{tlo, thi}, rw, 0, WS_EPOCH, AUX_SC1);
  STAMP(7);
}

}

extern "C" void kernel_launch(void* const* d_in, const int* in_sizes, int n_in, void* d_out, int out_size, void* d_ws,
                              size_t ws_size, hipStream_t stream) {
  (void)in_sizes; (void)n_in; (void)out_size; (void)ws_size;
  fused_kernel<<<dim3(NWG), dim3(NTHR), 0, stream>>>(
      (const char*)d_in[0], (const char*)d_in[1], (const char*)d_in[2], (const char*)d_in[3], (const char*)d_in[4],
      (const char*)d_in[6], (const char*)d_in[7], (const char*)d_in[8], (const char*)d_in[9], (char*)d_out, (char*)d_ws);
}
```

```cpp
#include <hip/hip_runtime.h>
#include <hip/hip_bf16.h>
#include <stdint.h>
#include <stdio.h>

namespace {

typedef unsigned u32x4 __attribute__((ext_vector_type(4)));
typedef unsigned u32x2 __attribute__((ext_vector_type(2)));
typedef float f32x2 __attribute__((ext_vector_type(2)));
typedef float f32x4 __attribute__((ext_vector_type(4)));
typedef __bf16 bf16x2 __attribute__((ext_vector_type(2)));

constexpr int D = 640, H = 32, SK = 8192, DH = 192, FF = 1536;
constexpr int NWG = 256, NTHR = 512, NWAVE = 8;
constexpr unsigned TILE_BYTES = 8 * DH * 2;
constexpr unsigned HEAD_BYTES = SK * DH * 2;
#define NOQ 0
#define NOWO 0
#define BURST 1
#define OUTAUX 0
#define PW 3
#define RA 8
#define PSLEEP 0
#define CATCH 3
#define RPRE 5
constexpr int GS = 4;
constexpr int NSW = 4, NH = NWAVE - NSW;
constexpr int STEPS = SK / 8 / (GS * NSW);
constexpr unsigned STRIDE = GS * NSW * TILE_BYTES;
constexpr int AUX_NT = 2, AUX_SC1 = 16;
constexpr unsigned RSRC3 = 0x00020000u;
constexpr unsigned OOB = 0x80000000u;

constexpr unsigned WS_EPOCH = 0;
constexpr unsigned WS_RDY = 4096;
constexpr unsigned WS_P = 8192;
constexpr unsigned WS_Y = WS_P + NWG * 1024;
constexpr unsigned WS_QP = WS_Y + 2 * 8 * 320 * 16;
constexpr unsigned WS_END = WS_QP + 32 * 2 * 8 * 96 * 16;

constexpr int L_RED = 0;
constexpr int L_ML = L_RED + NSW * DH;
constexpr int L_Q = L_ML + 16;
constexpr int L_TAG = L_Q + 96;
constexpr int L_CNT = L_TAG + 4;
constexpr int L_O = L_CNT + 8;
constexpr int L_YR = L_O + 384;
constexpr int L_H = L_YR + 320;
constexpr int L_ST = L_H + 320;
constexpr int L_MR = L_ST + 16;
constexpr int L_WO = L_MR + 64;
constexpr int L_TOTAL = 24 * 1024;
static_assert(L_WO + 24 * 2 * 64 * 4 <= L_TOTAL, "lds");
static_assert(L_WO % 4 == 0 && L_Q % 4 == 0 && L_ST % 4 == 0 && L_MR % 4 == 0, "alignment");

__device__ __forceinline__ float dot2(unsigned a, unsigned b, float c) {
  return __builtin_amdgcn_fdot2_f32_bf16(__builtin_bit_cast(bf16x2, a), __builtin_bit_cast(bf16x2, b), c, false);
}
template <int CTRL> __device__ __forceinline__ float dpp(float x) {
  return __builtin_bit_cast(float, __builtin_amdgcn_mov_dpp(__builtin_bit_cast(int, x), CTRL, 0xf, 0xf, true));
}
constexpr int XOR1 = 0xB1, XOR2 = 0x4E, HMIR = 0x141, ROR8 = 0x128;
constexpr unsigned ONES = 0x3f803f80u;
__device__ __forceinline__ float bflo(unsigned u) { return __builtin_bit_cast(float, u << 16); }
__device__ __forceinline__ float bfhi(unsigned u) { return __builtin_bit_cast(float, u & 0xffff0000u); }
__device__ __forceinline__ f32x2 bf2(unsigned u) { return f32x2{bflo(u), bfhi(u)}; }
__device__ __forceinline__ unsigned pack_bf16(float lo, float hi) {
  bf16x2 v; v[0] = (__bf16)lo; v[1] = (__bf16)hi;
  return __builtin_bit_cast(unsigned, v);
}
__device__ __forceinline__ float rbf(float x) {
  typedef float f32x2_ __attribute__((ext_vector_type(2)));
  return __builtin_bit_cast(float, __builtin_convertvector(f32x2_{0.f, x}, bf16x2));
}
__device__ __forceinline__ float fold32(float a, float b) {
  auto r = __builtin_amdgcn_permlane32_swap(__float_as_uint(a), __float_as_uint(b), false, false);
  return __uint_as_float(r[0]) + __uint_as_float(r[1]);
}
__device__ __forceinline__ float fold16(float a, float b) {
  auto r = __builtin_amdgcn_permlane16_swap(__float_as_uint(a), __float_as_uint(b), false, false);
  return __uint_as_float(r[0]) + __uint_as_float(r[1]);
}
__device__ __forceinline__ float fold8(float a, float b, bool bit3) {
  const float keep = bit3 ? b : a, send = bit3 ? a : b;
  return keep + dpp<ROR8>(send);
}
__device__ __forceinline__ float max3216(float x) {
  auto s = __builtin_amdgcn_permlane16_swap(__float_as_uint(x), __float_as_uint(x), false, false);
  x = fmaxf(__uint_as_float(s[0]), __uint_as_float(s[1]));
  auto t = __builtin_amdgcn_permlane32_swap(__float_as_uint(x), __float_as_uint(x), false, false);
  return fmaxf(__uint_as_float(t[0]), __uint_as_float(t[1]));
}
__device__ __forceinline__ float row_sum16(float x) {
  x += dpp<XOR1>(x); x += dpp<XOR2>(x); x += dpp<HMIR>(x); x += dpp<ROR8>(x);
  return x;
}
__device__ __forceinline__ unsigned long long mix64(unsigned long long z) {
  z += 0x9E3779B97F4A7C15ull;
  z = (z ^ (z >> 30)) * 0xBF58476D1CE4E5B9ull;
  z = (z ^ (z >> 27)) * 0x94D049BB133111EBull;
  return z ^ (z >> 31);
}
__device__ __forceinline__ u32x4 pack2(float a, float b, unsigned t8, unsigned thi) {
  u32x4 r; r.x = (__float_as_uint(a) >> 8) | t8; r.y = thi; r.z = (__float_as_uint(b) >> 8) | t8; r.w = thi; return r;
}
__device__ __forceinline__ unsigned tag_bad(u32x4 g, unsigned t8, unsigned thi) {
  return (((g.x ^ t8) | (g.z ^ t8)) >> 24) | (g.y ^ thi) | (g.w ^ thi);
}
__device__ __forceinline__ float g0(u32x4 g) { return __uint_as_float(g.x << 8); }
__device__ __forceinline__ float g1(u32x4 g) { return __uint_as_float(g.z << 8); }
__device__ __forceinline__ f32x2 g01(u32x4 g) { return f32x2{g0(g), g1(g)}; }
__device__ __forceinline__ unsigned vbit(float x) { return __float_as_uint(x) | 1u; }
__device__ __forceinline__ f32x2 asf2(unsigned a, unsigned b) { return f32x2{__uint_as_float(a), __uint_as_float(b)}; }
__device__ __forceinline__ float ln_den_inv(float var) {
  return __builtin_amdgcn_rcpf(rbf(__builtin_amdgcn_sqrtf(rbf(rbf(var) + 1e-5f))));
}
__device__ __forceinline__ float rdlane(float x, int l) { return __uint_as_float(__builtin_amdgcn_readlane(__float_as_uint(x), l)); }
__device__ __forceinline__ f32x2 splat(float x) { return f32x2{x, x}; }
__device__ __forceinline__ f32x2 fma2(f32x2 a, f32x2 b, f32x2 c) { return __builtin_elementwise_fma(a, b, c); }

#define SB() __builtin_amdgcn_sched_barrier(0)
#define MEMBAR() asm volatile("" ::: "memory")
#define LD128(rs, vo, so, aux) __builtin_amdgcn_raw_buffer_load_b128((rs), (vo), (so), (aux))
#define ST128(v, rs, vo, so) __builtin_amdgcn_raw_buffer_store_b128((v), (rs), (vo) + (so), 0, AUX_SC1)
#define LD64(rs, vo, so, aux) __builtin_amdgcn_raw_buffer_load_b64((rs), (vo), (so), (aux))
#define ST64(v, rs, vo, so) __builtin_amdgcn_raw_buffer_store_b64((v), (rs), (vo), (so), AUX_SC1)
#define WAIT_VM3() __builtin_amdgcn_s_waitcnt(0x0F73)
#define STAMP(i) do { } while (0)

constexpr unsigned SPIN_CAP = 1u << 17;

__global__ void __launch_bounds__(NTHR, 2)
fused_kernel(const char* x, const char* kc, const char* vc, const char* wq, const char* wo, const char* w1,
             const char* b1, const char* w2, const char* b2, char* out, char* ws) {
  __shared__ __attribute__((aligned(16))) float lds[L_TOTAL];
  unsigned* const ldu = reinterpret_cast<unsigned*>(lds);
  const int tid = threadIdx.x, lane = tid & 63, wave = __builtin_amdgcn_readfirstlane(tid >> 6);
  const int bid = blockIdx.x, team = bid & 7, jj = bid >> 3, hl = jj >> 3, b = (jj >> 2) & 1, sp = jj & 3;
  const int h = team * 4 + hl, cb = hl * 4 + sp, pq = jj & 7;
  const int r = lane >> 3, c = lane & 7;
  const bool bit3 = (lane & 8) != 0;
  const int fidx = 8 * c + 4 * (lane >> 5) + 2 * ((lane >> 4) & 1) + (bit3 ? 1 : 0);
  STAMP(0);
  asm volatile("" : "+s"(x), "+s"(kc), "+s"(vc), "+s"(wq), "+s"(wo), "+s"(w1), "+s"(b1), "+s"(w2), "+s"(b2), "+s"(out), "+s"(ws));
  const __amdgpu_buffer_rsrc_t rw = __builtin_amdgcn_make_buffer_rsrc((void*)ws, 0, WS_END, RSRC3);
  const __amdgpu_buffer_rsrc_t rx = __builtin_amdgcn_make_buffer_rsrc((void*)x, 0, 2 * D * 2, RSRC3);
  constexpr float SCALE = 0.07216878364870322f, LOG2E = 1.4426950408889634f;
  unsigned vzero;
  asm volatile("v_mov_b32 %0, 0" : "=v"(vzero));

  f32x2 fw1, fb1, fw2; float gw1, gb1, gw2, fb2;
  unsigned xres;
  unsigned short a1[3], a2[3], a3[3], a4;
#define LOAD_SMALL() { \
    const __amdgpu_buffer_rsrc_t r1 = __builtin_amdgcn_make_buffer_rsrc((void*)w1, 0, FF * 2 + 4, RSRC3); \
    const __amdgpu_buffer_rsrc_t r2 = __builtin_amdgcn_make_buffer_rsrc((void*)b1, 0, FF * 2 + 4, RSRC3); \
    const __amdgpu_buffer_rsrc_t r3 = __builtin_amdgcn_make_buffer_rsrc((void*)w2, 0, FF * 2 + 4, RSRC3); \
    _Pragma("unroll") for (int k = 0; k < 3; ++k) { \
      a1[k] = __builtin_amdgcn_raw_buffer_load_b16(r1, tid * 2 + 1024 * k, 0, 0); \
      a2[k] = __builtin_amdgcn_raw_buffer_load_b16(r2, tid * 2 + 1024 * k, 0, 0); \
      a3[k] = __builtin_amdgcn_raw_buffer_load_b16(r3, tid * 2 + 1024 * k, 0, 0); \
    } \
    a4 = __builtin_amdgcn_raw_buffer_load_b16(__builtin_amdgcn_make_buffer_rsrc((void*)b2, 0, 4, RSRC3), vzero, 0, 0); \
    xres = __builtin_amdgcn_raw_buffer_load_b32(rx, tid >= 192 ? (tid - 192) * 4 : OOB, b * D * 2, 0); \
    MEMBAR(); }
#define CONV_SMALL() { \
    fw1 = f32x2{bflo(a1[0]), bflo(a1[1])}; fb1 = f32x2{bflo(a2[0]), bflo(a2[1])}; fw2 = f32x2{bflo(a3[0]), bflo(a3[1])}; \
    gw1 = bflo(a1[2]); gb1 = bflo(a2[2]); gw2 = bflo(a3[2]); fb2 = bflo(a4); \
    asm volatile("" : "+v"(fw1), "+v"(fb1), "+v"(fw2), "+v"(gw1), "+v"(gb1), "+v"(gw2), "+v"(fb2), "+v"(xres) :: "memory"); }

  u32x4 wop[6][2];
  unsigned* const wl = ldu + L_WO + ((wave & 3) * 12 * 64 + lane) * 4;
  if (wave >= NH) {
    const int sw = wave - NH;
    const size_t kvbase = (size_t)(b * H + h) * HEAD_BYTES;
    const __amdgpu_buffer_rsrc_t rk = __builtin_amdgcn_make_buffer_rsrc((void*)(kc + kvbase), 0, HEAD_BYTES, RSRC3);
    const __amdgpu_buffer_rsrc_t rv = __builtin_amdgcn_make_buffer_rsrc((void*)(vc + kvbase), 0, HEAD_BYTES, RSRC3);
    const unsigned kvo = r * (DH * 2) + c * 16;
    u32x4 kbuf[RPRE][3], vbuf[RPRE][3];
    unsigned so = (sp * NSW + sw) * TILE_BYTES + (h & 1) * RA * STRIDE;
    static_assert(RA + RPRE + 2 < STEPS, "no wrap before the main loop");
#pragma unroll
    for (int j = 0; j < RPRE; ++j) {
      if (j >= BURST) { __builtin_amdgcn_s_waitcnt(0x0F70 | PW); SB(); }
#pragma unroll
      for (int t = 0; t < 3; ++t) { kbuf[j][t] = LD128(rk, kvo + t * 128, so, AUX_NT); SB(); }
      if (j >= BURST) { __builtin_amdgcn_s_waitcnt(0x0F70 | PW); SB(); }
#pragma unroll
      for (int t = 0; t < 3; ++t) { vbuf[j][t] = LD128(rv, kvo + t * 128, so, AUX_NT); SB(); }
      so += STRIDE;
    }
    LOAD_SMALL();
    STAMP(8);
    __syncthreads();
    u32x4 q[3];
#pragma unroll
    for (int t = 0; t < 3; ++t) q[t] = *reinterpret_cast<const u32x4*>(ldu + L_Q + (8 * t + c) * 4);
    STAMP(1);
    float m = -INFINITY, l = 0.f;
    f32x2 acc[12];
#pragma unroll
    for (int k = 0; k < 12; ++k) acc[k] = f32x2{0.f, 0.f};

#define PIN12(A) asm volatile("" : "+v"(A[0]), "+v"(A[1]), "+v"(A[2]), "+v"(A[3]), "+v"(A[4]), "+v"(A[5]), "+v"(A[6]), "+v"(A[7]), \
    "+v"(A[8]), "+v"(A[9]), "+v"(A[10]), "+v"(A[11]) :: "memory")
#define STEP(KB, VB, REFILL) { \
    float s0_ = 0.f, s1_ = 0.f, s2_ = 0.f; \
    _Pragma("unroll") for (int j_ = 0; j_ < 4; ++j_) { s0_ = dot2(KB[0][j_], q[0][j_], s0_); s1_ = dot2(KB[1][j_], q[1][j_], s1_); s2_ = dot2(KB[2][j_], q[2][j_], s2_); } \
    asm volatile("" : "+v"(s0_), "+v"(s1_), "+v"(s2_) :: "memory"); \
    SB(); \
    if (REFILL) { _Pragma("unroll") for (int u_ = 0; u_ < 3; ++u_) KB[u_] = LD128(rk, kvo + u_ * 128, so, AUX_NT); } \
    SB(); \
    float s_ = s0_ + s1_ + s2_; \
    s_ += dpp<XOR1>(s_); s_ += dpp<XOR2>(s_); s_ += dpp<HMIR>(s_); \
    s_ = rbf(rbf(s_) * SCALE) * LOG2E; \
    if (__any(s_ > m)) { \
      const float mn_ = fmaxf(m, max3216(fmaxf(s_, dpp<ROR8>(s_)))); \
      const float al_ = __builtin_amdgcn_exp2f(m - mn_); \
      m = mn_; l *= al_; \
      _Pragma("unroll") for (int k_ = 0; k_ < 12; ++k_) acc[k_] *= splat(al_); \
    } \
    float p_ = __builtin_amdgcn_exp2f(s_ - m); \
    asm volatile("" : "+v"(p_)); \
    SB(); \
    f32x2 vf_[12]; \
    _Pragma("unroll") for (int t_ = 0; t_ < 3; ++t_) \
      _Pragma("unroll") for (int j_ = 0; j_ < 4; ++j_) vf_[t_ * 4 + j_] = bf2(VB[t_][j_]); \
    PIN12(vf_); \
    SB(); \
    if (REFILL) { _Pragma("unroll") for (int u_ = 0; u_ < 3; ++u_) VB[u_] = LD128(rv, kvo + u_ * 128, so, AUX_NT); } \
    SB(); \
    l += p_; \
    _Pragma("unroll") for (int k_ = 0; k_ < 12; ++k_) acc[k_] = fma2(splat(p_), vf_[k_], acc[k_]); \
    PIN12(acc); \
    SB(); }

#define STEP2(KB, VB, KN, VN) { \
    __builtin_amdgcn_s_waitcnt(0x0F73); \
    SB(); \
    _Pragma("unroll") for (int u_ = 0; u_ < 3; ++u_) KN[u_] = LD128(rk, kvo + u_ * 128, so, AUX_NT); \
    MEMBAR(); \
    SB(); \
    float s0_ = 0.f, s1_ = 0.f, s2_ = 0.f; \
    _Pragma("unroll") for (int j_ = 0; j_ < 4; ++j_) { s0_ = dot2(KB[0][j_], q[0][j_], s0_); s1_ = dot2(KB[1][j_], q[1][j_], s1_); s2_ = dot2(KB[2][j_], q[2][j_], s2_); } \
    float s_ = s0_ + s1_ + s2_; \
    s_ += dpp<XOR1>(s_); s_ += dpp<XOR2>(s_); s_ += dpp<HMIR>(s_); \
    s_ = rbf(rbf(s_) * SCALE) * LOG2E; \
    if (__any(s_ > m)) { \
      const float mn_ = fmaxf(m, max3216(fmaxf(s_, dpp<ROR8>(s_)))); \
      const float al_ = __builtin_amdgcn_exp2f(m - mn_); \
      m = mn_; l *= al_; \
      _Pragma("unroll") for (int k_ = 0; k_ < 12; ++k_) acc[k_] *= splat(al_); \
    } \
    float p_ = __builtin_amdgcn_exp2f(s_ - m); \
    asm volatile("" : "+v"(p_) :: "memory"); \
    SB(); \
    __builtin_amdgcn_s_waitcnt(0x0F73); \
    SB(); \
    _Pragma("unroll") for (int u_ = 0; u_ < 3; ++u_) VN[u_] = LD128(rv, kvo + u_ * 128, so, AUX_NT); \
    MEMBAR(); \
    SB(); \
    l += p_; \
    _Pragma("unroll") for (int t_ = 0; t_ < 3; ++t_) \
      _Pragma("unroll") for (int j_ = 0; j_ < 4; ++j_) acc[t_ * 4 + j_] = fma2(splat(p_), bf2(VB[t_][j_]), acc[t_ * 4 + j_]); \
    PIN12(acc); \
    SB(); }

    static_assert(RPRE % 2 == 1 && CATCH >= 1 && CATCH < RPRE, "parity of the main loop");
    STEP(kbuf[0], vbuf[0], true);
#pragma unroll
    for (int jb = 1; jb <= CATCH; ++jb) STEP(kbuf[jb], vbuf[jb], false);
    so += STRIDE;
    WAIT_VM3(); SB();
#pragma unroll
    for (int t = 0; t < 3; ++t) kbuf[1][t] = LD128(rk, kvo + t * 128, so, AUX_NT);
    SB(); WAIT_VM3(); SB();
#pragma unroll
    for (int t = 0; t < 3; ++t) vbuf[1][t] = LD128(rv, kvo + t * 128, so, AUX_NT);
    MEMBAR(); SB();
#pragma unroll
    for (int jb = CATCH + 1; jb < RPRE; ++jb) STEP(kbuf[jb], vbuf[jb], false);
    STEP(kbuf[0], vbuf[0], false);
#pragma nounroll
    for (int i = RPRE + 1; i < STEPS - 2; i += 2) {
      so += STRIDE;
      if (so >= HEAD_BYTES) so -= HEAD_BYTES;
      STEP2(kbuf[1], vbuf[1], kbuf[0], vbuf[0]);
      so += STRIDE;
      if (so >= HEAD_BYTES) so -= HEAD_BYTES;
      STEP2(kbuf[0], vbuf[0], kbuf[1], vbuf[1]);
    }
    so += STRIDE;
    if (so >= HEAD_BYTES) so -= HEAD_BYTES;
    STEP2(kbuf[1], vbuf[1], kbuf[0], vbuf[0]);
    STEP(kbuf[0], vbuf[0], false);
    STAMP(2);
    {
      float lw = l + dpp<ROR8>(l);
      lw = fold16(lw, lw); lw = fold32(lw, lw);
      float* const red = lds + L_RED + sw * DH + fidx;
#pragma unroll
      for (int t = 0; t < 3; ++t) {
        const float z0 = fold32(acc[t * 4 + 0].x, acc[t * 4 + 2].x), z1 = fold32(acc[t * 4 + 0].y, acc[t * 4 + 2].y);
        const float z2 = fold32(acc[t * 4 + 1].x, acc[t * 4 + 3].x), z3 = fold32(acc[t * 4 + 1].y, acc[t * 4 + 3].y);
        red[64 * t] = fold8(fold16(z0, z2), fold16(z1, z3), bit3);
      }
      unsigned arrived = 0;
      if (lane == 0) {
        *reinterpret_cast<f32x2*>(lds + L_ML + 2 * sw) = f32x2{m, lw};
        arrived = __hip_atomic_fetch_add(ldu + L_CNT, 1u, __ATOMIC_ACQ_REL, __HIP_MEMORY_SCOPE_WORKGROUP);
      }
      if (__builtin_amdgcn_readfirstlane(arrived) == NSW - 1) {
        const int d4 = lane < 48 ? 4 * lane : 0;
        const f32x4 ml01 = *reinterpret_cast<const f32x4*>(lds + L_ML), ml23 = *reinterpret_cast<const f32x4*>(lds + L_ML + 4);
        f32x4 v[NSW];
#pragma unroll
        for (int w = 0; w < NSW; ++w) v[w] = *reinterpret_cast<const f32x4*>(lds + L_RED + w * DH + d4);
        const float M = __uint_as_float(vbit(fmaxf(fmaxf(ml01.x, ml01.z), fmaxf(ml23.x, ml23.z))));
        const float f0 = __builtin_amdgcn_exp2f(ml01.x - M), f1 = __builtin_amdgcn_exp2f(ml01.z - M);
        const float f2 = __builtin_amdgcn_exp2f(ml23.x - M), f3 = __builtin_amdgcn_exp2f(ml23.z - M);
        f32x4 o = (v[0] * f0 + v[1] * f1) + (v[2] * f2 + v[3] * f3);
        const float Ls = (ml01.y * f0 + ml01.w * f1) + (ml23.y * f2 + ml23.w * f3);
        if (lane >= 48) { o.x = M; o.y = Ls; }
        ST128((u32x4{vbit(o.x), vbit(o.y), vbit(o.z), vbit(o.w)}), rw, lane <= 48 ? lane * 16 : OOB, WS_P + bid * 1024);
      }
    }
    STAMP(3);
  } else {
    if (wave < 3 && !NOQ) {
      const __amdgpu_buffer_rsrc_t rq = __builtin_amdgcn_make_buffer_rsrc((void*)wq, 0, D * H * DH * 2, RSRC3);
      unsigned xs[2][5], xp[2];
#pragma unroll
      for (int bb = 0; bb < 2; ++bb) {
#pragma unroll
        for (int i = 0; i < 5; ++i) xs[bb][i] = __builtin_amdgcn_raw_buffer_load_b32(rx, lane * 4 + i * 256 + bb * D * 2, 0, 0);
        xp[bb] = __builtin_amdgcn_raw_buffer_load_b32(rx, (c < 5 ? (8 * c + r) * 4 : 0) + bb * D * 2, pq * 160, 0);
      }
      const u32x2 ep = __builtin_amdgcn_raw_buffer_load_b64(rw, vzero, WS_EPOCH, AUX_SC1);
      SB();
      u32x4 wqr[5][2];
      {
        const unsigned vo = (unsigned)(2 * r) * (H * DH * 2) + c * 16;
        const unsigned qso = (unsigned)(80 * pq) * (H * DH * 2) + h * (DH * 2) + wave * 128;
#pragma unroll
        for (int i = 0; i < 5; ++i)
#pragma unroll
          for (int e = 0; e < 2; ++e) { wqr[i][e] = LD128(rq, vo, qso + (16 * i + e) * (H * DH * 2), AUX_NT); SB(); }
      }
      float s0 = 0.f, s1 = 0.f, q0 = 0.f, q1 = 0.f;
#pragma unroll
      for (int i = 0; i < 5; ++i) {
        s0 = dot2(xs[0][i], ONES, s0); q0 = dot2(xs[0][i], xs[0][i], q0);
        s1 = dot2(xs[1][i], ONES, s1); q1 = dot2(xs[1][i], xs[1][i], q1);
      }
      const float st = row_sum16(fold16(fold32(s0, s1), fold32(q0, q1)));
      auto sw2 = __builtin_amdgcn_permlane16_swap(__float_as_uint(st), __float_as_uint(st), false, false);
      const float mean = __uint_as_float(sw2[0]) * (1.0f / D);
      const float var = fmaxf(fmaf(-mean, mean, __uint_as_float(sw2[1]) * (1.0f / D)), 0.f);
      const float inv = ln_den_inv(var), mb = rbf(mean);
      const float m0 = rdlane(mb, 0), m1 = rdlane(mb, 32), i0 = rdlane(inv, 0), i1 = rdlane(inv, 32);
      const unsigned xn0 = pack_bf16(rbf(bflo(xp[0]) - m0) * i0, rbf(bfhi(xp[0]) - m0) * i0);
      const unsigned xn1 = pack_bf16(rbf(bflo(xp[1]) - m1) * i1, rbf(bfhi(xp[1]) - m1) * i1);
      const unsigned long long tg = mix64(((unsigned long long)(unsigned)__builtin_amdgcn_readfirstlane(ep.y) << 32) | (unsigned)__builtin_amdgcn_readfirstlane(ep.x));
      const unsigned thi = (unsigned)(tg >> 32), t8 = (unsigned)tg & 0xff000000u;
      float a0[8], a1q[8];
#pragma unroll
      for (int k = 0; k < 8; ++k) { a0[k] = 0.f; a1q[k] = 0.f; }
#define QGROUP(I) { \
        unsigned b0_ = __builtin_amdgcn_update_dpp(0u, xn0, 0x150 + I, 0xf, 0x3, false); b0_ = __builtin_amdgcn_update_dpp(b0_, xn0, 0x158 + I, 0xf, 0xc, false); \
        unsigned b1_ = __builtin_amdgcn_update_dpp(0u, xn1, 0x150 + I, 0xf, 0x3, false); b1_ = __builtin_amdgcn_update_dpp(b1_, xn1, 0x158 + I, 0xf, 0xc, false); \
        _Pragma("unroll") for (int j = 0; j < 4; ++j) { \
          const unsigned plo = __builtin_amdgcn_perm(wqr[I][1][j], wqr[I][0][j], 0x05040100u), phi = __builtin_amdgcn_perm(wqr[I][1][j], wqr[I][0][j], 0x07060302u); \
          a0[2 * j] = dot2(plo, b0_, a0[2 * j]); a0[2 * j + 1] = dot2(phi, b0_, a0[2 * j + 1]); \
          a1q[2 * j] = dot2(plo, b1_, a1q[2 * j]); a1q[2 * j + 1] = dot2(phi, b1_, a1q[2 * j + 1]); } }
      QGROUP(0) QGROUP(1) QGROUP(2) QGROUP(3) QGROUP(4)
      float z[8];
#pragma unroll
      for (int k = 0; k < 8; ++k) z[k] = fold32(a0[k], a1q[k]);
      float y[4];
#pragma unroll
      for (int k = 0; k < 4; ++k) y[k] = fold16(z[k], z[k + 4]);
      const float u0 = fold8(y[0], y[2], bit3), u1 = fold8(y[1], y[3], bit3);
      const unsigned pidx = 32 * wave + 4 * c + 2 * ((lane >> 4) & 1) + (bit3 ? 1 : 0);
      ST128(pack2(u0, u1, t8, thi), rw, ((lane >> 5) * 8 * 96 + pidx) * 16, WS_QP + (h * 16 + pq) * (96 * 16));
      STAMP(8);
      asm volatile("s_waitcnt vmcnt(0)" ::: "memory");
      {
        const unsigned qo = ((lane >> 5) * 96 + 32 * wave + (lane & 31)) * 16;
        const unsigned qb = WS_QP + (h * 16 + b * 8) * (96 * 16);
        u32x4 gq[4];
        for (unsigned spins = 0;; ++spins) {
          MEMBAR();
#pragma unroll
          for (int k = 0; k < 4; ++k) gq[k] = LD128(rw, qo, qb + k * (2 * 96 * 16), AUX_SC1);
          unsigned bad = 0;
#pragma unroll
          for (int k = 0; k < 4; ++k) bad |= tag_bad(gq[k], t8, thi);
          if (__all(bad == 0u) || spins > SPIN_CAP) break;
        }
        const f32x2 qq = (g01(gq[0]) + g01(gq[1])) + (g01(gq[2]) + g01(gq[3]));
        const float qa = fold32(qq.x, qq.x), qc = fold32(qq.y, qq.y);
        if (lane < 32) ldu[L_Q + 32 * wave + lane] = pack_bf16(qa, qc);
      }
      if (tid == 0) *reinterpret_cast<u32x2*>(ldu + L_TAG) = u32x2{(unsigned)tg, thi};
    }
    const unsigned yoff = ((lane >= 20 ? 8 * 320 : 0) + (lane >= 20 ? lane - 20 : lane)) * 16;
    const unsigned ybo = WS_Y + (team * 320 + cb * 20) * 16 + b * 8;
    if (wave == 3) {
      if (lane == 0) ldu[L_CNT] = 0u;
      const u32x2 ep = LD64(rw, vzero, WS_EPOCH, AUX_SC1);
      ST128((u32x4{0u, 0u, 0u, 0u}), rw, lane * 16, WS_P + bid * 1024);
      ST64((u32x2{0u, 0u}), rw, lane < 40 ? yoff : OOB, ybo);
      asm volatile("s_waitcnt vmcnt(0)" ::: "memory");
      const unsigned long long tg = mix64(((unsigned long long)(unsigned)__builtin_amdgcn_readfirstlane(ep.y) << 32) | (unsigned)__builtin_amdgcn_readfirstlane(ep.x));
      ST64((u32x2{(unsigned)tg, (unsigned)(tg >> 32)}), rw, lane == 0 ? 0u : OOB, WS_RDY + (team * 32 + jj) * 8);
    }
    if (NOQ && wave == 0) {
      const u32x2 ep = __builtin_amdgcn_raw_buffer_load_b64(rw, vzero, WS_EPOCH, AUX_SC1);
      const unsigned long long tg = mix64(((unsigned long long)(unsigned)__builtin_amdgcn_readfirstlane(ep.y) << 32) | (unsigned)__builtin_amdgcn_readfirstlane(ep.x));
      if (tid == 0) *reinterpret_cast<u32x2*>(ldu + L_TAG) = u32x2{(unsigned)tg, (unsigned)(tg >> 32)};
      ldu[L_Q + lane] = 0x3c003c00; ldu[L_Q + 32 + lane] = 0x3c003c00;
    }
    STAMP(9);
    __syncthreads();
    STAMP(1);
    LOAD_SMALL();
    const __amdgpu_buffer_rsrc_t ro = __builtin_amdgcn_make_buffer_rsrc((void*)wo, 0, D * H * DH * 2, RSRC3);
    const unsigned vo = c < 5 ? (unsigned)(2 * r) * (D * 2) + c * 16 : OOB;
    const unsigned wso = NOWO ? OOB : (unsigned)(768 * team + 384 * b + 96 * wave) * (D * 2) + cb * 80;
#pragma unroll
    for (int i = 0; i < 6; ++i) {
      const u32x4 e0 = LD128(ro, vo, wso + 16 * i * (D * 2), AUX_NT);
      const u32x4 e1 = LD128(ro, vo + D * 2, wso + 16 * i * (D * 2), AUX_NT);
      u32x4 p0, p1;
      p0.x = __builtin_amdgcn_perm(e1.x, e0.x, 0x05040100u); p0.y = __builtin_amdgcn_perm(e1.x, e0.x, 0x07060302u);
      p0.z = __builtin_amdgcn_perm(e1.y, e0.y, 0x05040100u); p0.w = __builtin_amdgcn_perm(e1.y, e0.y, 0x07060302u);
      p1.x = __builtin_amdgcn_perm(e1.z, e0.z, 0x05040100u); p1.y = __builtin_amdgcn_perm(e1.z, e0.z, 0x07060302u);
      p1.z = __builtin_amdgcn_perm(e1.w, e0.w, 0x05040100u); p1.w = __builtin_amdgcn_perm(e1.w, e0.w, 0x07060302u);
      wop[i][0] = p0; wop[i][1] = p1;
      *reinterpret_cast<u32x4*>(wl + (2 * i) * 256) = p0;
      *reinterpret_cast<u32x4*>(wl + (2 * i + 1) * 256) = p1;
      MEMBAR();
      SB();
    }
    if (wave == 0) {
      const u32x2 tg = *reinterpret_cast<const u32x2*>(ldu + L_TAG);
      for (unsigned spins = 0; spins < SPIN_CAP; ++spins) {
        MEMBAR();
        const u32x4 r0 = LD128(rw, lane * 16, WS_RDY, AUX_SC1), r1 = LD128(rw, lane * 16 + 1024, WS_RDY, AUX_SC1);
        const unsigned bad = (r0.x ^ tg.x) | (r0.y ^ tg.y) | (r0.z ^ tg.x) | (r0.w ^ tg.y) | (r1.x ^ tg.x) | (r1.y ^ tg.y) | (r1.z ^ tg.x) | (r1.w ^ tg.y);
        if (__all(bad == 0u)) break;
        __builtin_amdgcn_s_sleep(32);
      }
    }
  }
  const u32x2 tg2 = *reinterpret_cast<const u32x2*>(ldu + L_TAG);
  const unsigned tlo = __builtin_amdgcn_readfirstlane(tg2.x), thi = __builtin_amdgcn_readfirstlane(tg2.y);

  __syncthreads();
  {
    const unsigned pbase = WS_P + (unsigned)(team + 8 * ((2 * b + ((wave >> 1) & 1)) * 8 + (wave >> 2) * 4)) * 1024;
    const unsigned po = (lane < 48 ? (wave & 1) * 48 + lane : 96) * 8;
    u32x2 go[GS];
    MEMBAR();
#pragma unroll
    for (int s = 0; s < GS; ++s) go[s] = LD64(rw, po, pbase + s * (8 * 1024), AUX_SC1);
    MEMBAR();
    SB();
    if (wave >= NH) {
#pragma unroll
      for (int i = 0; i < 6; ++i) {
        wop[i][0] = *reinterpret_cast<const u32x4*>(wl + (2 * i) * 256);
        wop[i][1] = *reinterpret_cast<const u32x4*>(wl + (2 * i + 1) * 256);
      }
    }
    MEMBAR();
    unsigned pend = 0, spins = 0;
#pragma unroll
    for (int s = 0; s < GS; ++s)
      if (!__all((go[s].x & go[s].y & 1u) != 0u)) pend |= 1u << s;
    while (pend != 0u && ++spins < SPIN_CAP) {
      MEMBAR();
#pragma unroll
      for (int s = 0; s < GS; ++s)
        if (pend & (1u << s)) go[s] = LD64(rw, po, pbase + s * (8 * 1024), AUX_SC1);
#pragma unroll
      for (int s = 0; s < GS; ++s)
        if (pend & (1u << s)) { if (__all((go[s].x & go[s].y & 1u) != 0u)) pend &= ~(1u << s); }
    }
    const int sl = lane & 3;
    const u32x2 gm = sl == 0 ? go[0] : sl == 1 ? go[1] : sl == 2 ? go[2] : go[3];
    const float ms = __uint_as_float(gm.x);
    float M = fmaxf(ms, dpp<XOR1>(ms)); M = fmaxf(M, dpp<XOR2>(M));
    const float f = __builtin_amdgcn_exp2f(ms - M);
    float Ls = f * __uint_as_float(gm.y);
    Ls += dpp<XOR1>(Ls); Ls += dpp<XOR2>(Ls);
    const float wgt = f * __builtin_amdgcn_rcpf(Ls);
    f32x2 oa = splat(rdlane(wgt, 48)) * asf2(go[0].x, go[0].y), ob = splat(rdlane(wgt, 49)) * asf2(go[1].x, go[1].y);
    oa = fma2(splat(rdlane(wgt, 50)), asf2(go[2].x, go[2].y), oa); ob = fma2(splat(rdlane(wgt, 51)), asf2(go[3].x, go[3].y), ob);
    oa += ob;
    if (lane < 48) ldu[L_O + 48 * wave + lane] = pack_bf16(oa.x, oa.y);
  }
  STAMP(4);
  {
    unsigned op[6];
#pragma unroll
    for (int i = 0; i < 6; ++i) op[i] = ldu[L_O + 48 * wave + 8 * i + r];
    float y[8];
#pragma unroll
    for (int k = 0; k < 8; ++k) y[k] = 0.f;
#pragma unroll
    for (int i = 0; i < 6; ++i) {
#pragma unroll
      for (int k = 0; k < 4; ++k) { y[k] = dot2(wop[i][0][k], op[i], y[k]); y[k + 4] = dot2(wop[i][1][k], op[i], y[k + 4]); }
    }
    const float z0 = fold32(y[0], y[4]), z1 = fold32(y[1], y[5]), z2 = fold32(y[2], y[6]), z3 = fold32(y[3], y[7]);
    const float u = fold8(fold16(z0, z2), fold16(z1, z3), bit3);
    if (c < 5) lds[L_YR + wave * 40 + fidx] = u;
  }
  __syncthreads();
  if (tid < 40) {
    const int bb = tid >= 20, t = tid - 20 * bb;
    f32x2 v[4];
#pragma unroll
    for (int w = 0; w < 4; ++w) v[w] = *reinterpret_cast<const f32x2*>(lds + L_YR + (4 * bb + w) * 40 + 2 * t);
    const f32x2 ys = (v[0] + v[1]) + (v[2] + v[3]);
    ST64((u32x2{vbit(ys.x), vbit(ys.y)}), rw, (bb * 8 * 320 + t) * 16, WS_Y + (team * 320 + cb * 20) * 16 + b * 8);
  }
  STAMP(5);
  __syncthreads();
  CONV_SMALL();
  if (wave >= 3) {
    u32x4 gy[8];
    const unsigned yo = (tid - 192) * 16, ybase = WS_Y + b * (8 * 320 * 16);
    unsigned pend = 0xffu, spins = 0;
    do {
      MEMBAR();
#pragma unroll
      for (int s = 0; s < 8; ++s)
        if (pend & (1u << s)) gy[s] = LD128(rw, yo, ybase + s * (320 * 16), AUX_SC1);
#pragma unroll
      for (int s = 0; s < 8; ++s)
        if (pend & (1u << s)) { if (__all((gy[s].x & gy[s].y & gy[s].z & gy[s].w & 1u) != 0u)) pend &= ~(1u << s); }
      if (PSLEEP && pend != 0u) __builtin_amdgcn_s_sleep(PSLEEP);
    } while (pend != 0u && ++spins < SPIN_CAP);
    f32x2 ap[8];
#pragma unroll
    for (int s = 0; s < 8; ++s) ap[s] = asf2(gy[s].x, gy[s].y) + asf2(gy[s].z, gy[s].w);
    const f32x2 a = ((ap[0] + ap[1]) + (ap[2] + ap[3])) + ((ap[4] + ap[5]) + (ap[6] + ap[7]));
    const unsigned hp = pack_bf16(bflo(xres) + rbf(a.x), bfhi(xres) + rbf(a.y));
    ldu[L_H + tid - 192] = hp;
    float st = fold32(dot2(hp, ONES, 0.f), dot2(hp, hp, 0.f));
    st = row_sum16(fold16(st, st));
    if ((lane & 31) == 0) lds[L_ST + (wave - 3) * 2 + (lane >> 5)] = st;
  }
  __syncthreads();
  STAMP(6);
  const int d0 = 5 * (team * 16 + cb);
  float he;
  {
    const int de = d0 + (lane < 5 ? lane : 0);
    const unsigned hw = ldu[L_H + (de >> 1)];
    f32x2 sv[5];
#pragma unroll
    for (int w = 0; w < 5; ++w) sv[w] = *reinterpret_cast<const f32x2*>(lds + L_ST + 2 * w);
    const f32x2 st = (((sv[0] + sv[1]) + (sv[2] + sv[3])) + sv[4]) * splat(1.0f / D);
    he = (de & 1) ? bfhi(hw) : bflo(hw);
    const float mean1 = st.x;
    const float var1 = fmaxf(fmaf(-mean1, mean1, st.y), 0.f);
    const float i1 = ln_den_inv(var1), m1 = rbf(mean1);
    const float m2 = rbf((mean1 - m1) * i1), i2 = ln_den_inv(var1 * i1 * i1);
    const float tl = rbf(rbf(rbf(rbf(he - m1) * i1) - m2) * i2);
    constexpr float G1 = -2.0f * 0.7978845608028654f * LOG2E, G3 = G1 * 0.044715f;
    float te[5];
#pragma unroll
    for (int e = 0; e < 5; ++e) te[e] = rdlane(tl, e);
    f32x2 u[8], z[8];
#pragma unroll
    for (int e = 0; e < 5; ++e) u[e] = fma2(splat(te[e]), fw1, fb1);
    u[5] = fma2(f32x2{te[0], te[1]}, splat(gw1), splat(gb1));
    u[6] = fma2(f32x2{te[2], te[3]}, splat(gw1), splat(gb1));
    u[7] = fma2(splat(te[4]), splat(gw1), splat(gb1));
    SB();
#pragma unroll
    for (int k = 0; k < 8; ++k) z[k] = u[k] * u[k];
    SB();
#pragma unroll
    for (int k = 0; k < 8; ++k) z[k] = fma2(z[k], splat(G3), splat(G1));
    SB();
#pragma unroll
    for (int k = 0; k < 8; ++k) z[k] = z[k] * u[k];
    SB();
#pragma unroll
    for (int k = 0; k < 8; ++k) { z[k].x = __builtin_amdgcn_exp2f(z[k].x); if (k < 7) z[k].y = __builtin_amdgcn_exp2f(z[k].y); }
#pragma unroll
    for (int k = 0; k < 8; ++k) u[k] = u[k] * (k < 5 ? fw2 : splat(gw2));
    SB();
#pragma unroll
    for (int k = 0; k < 8; ++k) z[k] = z[k] + splat(1.0f);
    SB();
#pragma unroll
    for (int k = 0; k < 8; ++k) { z[k].x = __builtin_amdgcn_rcpf(z[k].x); if (k < 7) z[k].y = __builtin_amdgcn_rcpf(z[k].y); }
    SB();
#pragma unroll
    for (int k = 0; k < 8; ++k) u[k] = u[k] * z[k];
    const float ya0 = (u[0].x + u[0].y) + u[5].x, ya1 = (u[1].x + u[1].y) + u[5].y, ya2 = (u[2].x + u[2].y) + u[6].x,
                ya3 = (u[3].x + u[3].y) + u[6].y, ya4 = (u[4].x + u[4].y) + u[7].x;
    const float f4 = fold32(ya4, ya4);
    float r0 = fold8(fold16(fold32(ya0, ya1), fold32(ya2, ya3)), fold16(f4, f4), bit3);
    r0 += dpp<XOR1>(r0); r0 += dpp<XOR2>(r0); r0 += dpp<HMIR>(r0);
    const int el = bit3 ? 4 : (lane >> 5) + 2 * ((lane >> 4) & 1);
    if ((lane & 15) == 0 || lane == 8) lds[L_MR + el * 8 + wave] = r0;
  }
  __syncthreads();
  if (tid < 5) {
    const f32x4 pa = *reinterpret_cast<const f32x4*>(lds + L_MR + tid * 8), pb = *reinterpret_cast<const f32x4*>(lds + L_MR + tid * 8 + 4);
    const f32x4 ps = pa + pb;
    const float yv = (ps.x + ps.y) + (ps.z + ps.w);
    const unsigned o = pack_bf16(he + rbf(rbf(yv) + fb2), 0.f);
    __builtin_amdgcn_raw_buffer_store_b16((unsigned short)o, __builtin_amdgcn_make_buffer_rsrc((void*)out, 0, 2 * D * 2 + 4, RSRC3),
                                          tid * 2, (b * D + d0) * 2, OUTAUX);
  }
  if (tid == 0 && bid == 0)
    __builtin_amdgcn_raw_buffer_store_b64(u32x2{tlo, thi}, rw, 0, WS_EPOCH, AUX_SC1);
  STAMP(7);
}

}

extern "C" void kernel_launch(void* const* d_in, const int* in_sizes, int n_in, void* d_out, int out_size, void* d_ws,
                              size_t ws_size, hipStream_t stream) {
  for (int i = 0; i < n_in; ++i) fprintf(stderr, "in%d %p (%d)\n", i, d_in[i], in_sizes[i]);
  fprintf(stderr, "out %p ws %p %zu\n", d_out, d_ws, ws_size);
  { hipPointerAttribute_t at; if (hipPointerGetAttributes(&at, d_in[1]) == hipSuccess) fprintf(stderr, "type %d managed %d flags %u\n", (int)at.type, at.isManaged, at.allocationFlags); void* base; size_t sz; if (hipMemGetAddressRange(&base, &sz, d_in[1]) == hipSuccess) fprintf(stderr, "K range %p %zu\n", base, sz); }
  fused_kernel<<<dim3(NWG), dim3(NTHR), 0, stream>>>(
      (const char*)d_in[0], (const char*)d_in[1], (const char*)d_in[2], (const char*)d_in[3], (const char*)d_in[4],
      (const char*)d_in[6], (const char*)d_in[7], (const char*)d_in[8], (const char*)d_in[9], (char*)d_out, (char*)d_ws);
}
```

```cpp
#include <hip/hip_runtime.h>
#include <hip/hip_bf16.h>
#include <stdint.h>

namespace {

typedef unsigned u32x4 __attribute__((ext_vector_type(4)));
typedef unsigned u32x2 __attribute__((ext_vector_type(2)));
typedef float f32x2 __attribute__((ext_vector_type(2)));
typedef float f32x4 __attribute__((ext_vector_type(4)));
typedef __bf16 bf16x2 __attribute__((ext_vector_type(2)));

constexpr int D = 640, H = 32, SK = 8192, DH = 192, FF = 1536;
constexpr int NWG = 256, NTHR = 512, NWAVE = 8;
constexpr unsigned TILE_BYTES = 8 * DH * 2;
constexpr unsigned HEAD_BYTES = SK * DH * 2;
#define NOQ 0
#define NOWO 0
#define RA 8
#define RPRE 10
constexpr int GS = 4;
constexpr int NSW = 4, NH = NWAVE - NSW;
constexpr int STEPS = SK / 8 / (GS * NSW);
constexpr unsigned STRIDE = GS * NSW * TILE_BYTES;
constexpr int AUX_NT = 2, AUX_SC1 = 16;
constexpr unsigned RSRC3 = 0x00020000u;
constexpr unsigned OOB = 0x80000000u;

constexpr unsigned WS_EPOCH = 0;
constexpr unsigned WS_RDY = 4096;
constexpr unsigned WS_P = 8192;
constexpr unsigned WS_Y = WS_P + NWG * 1024;
constexpr unsigned WS_QP = WS_Y + 2 * 8 * 320 * 16;
constexpr unsigned WS_END = WS_QP + 32 * 2 * 8 * 96 * 16;

constexpr int L_RED = 0;
constexpr int L_ML = L_RED + NSW * DH;
constexpr int L_Q = L_ML + 16;
constexpr int L_TAG = L_Q + 96;
constexpr int L_CNT = L_TAG + 4;
constexpr int L_O = L_CNT + 8;
constexpr int L_YR = L_O + 384;
constexpr int L_H = L_YR + 320;
constexpr int L_ST = L_H + 320;
constexpr int L_MR = L_ST + 16;
constexpr int L_SC = L_MR + 64;
constexpr int L_WO = L_SC + NSW * 512;
constexpr int L_TOTAL = 24 * 1024;
static_assert(L_WO + 24 * 2 * 64 * 4 <= L_TOTAL, "lds");
static_assert(L_WO % 4 == 0 && L_Q % 4 == 0 && L_ST % 4 == 0 && L_MR % 4 == 0, "alignment");

__device__ __forceinline__ float dot2(unsigned a, unsigned b, float c) {
  return __builtin_amdgcn_fdot2_f32_bf16(__builtin_bit_cast(bf16x2, a), __builtin_bit_cast(bf16x2, b), c, false);
}
template <int CTRL> __device__ __forceinline__ float dpp(float x) {
  return __builtin_bit_cast(float, __builtin_amdgcn_mov_dpp(__builtin_bit_cast(int, x), CTRL, 0xf, 0xf, true));
}
constexpr int XOR1 = 0xB1, XOR2 = 0x4E, HMIR = 0x141, ROR8 = 0x128;
constexpr unsigned ONES = 0x3f803f80u;
__device__ __forceinline__ float bflo(unsigned u) { return __builtin_bit_cast(float, u << 16); }
__device__ __forceinline__ float bfhi(unsigned u) { return __builtin_bit_cast(float, u & 0xffff0000u); }
__device__ __forceinline__ f32x2 bf2(unsigned u) { return f32x2{bflo(u), bfhi(u)}; }
__device__ __forceinline__ unsigned pack_bf16(float lo, float hi) {
  bf16x2 v; v[0] = (__bf16)lo; v[1] = (__bf16)hi;
  return __builtin_bit_cast(unsigned, v);
}
__device__ __forceinline__ float rbf(float x) {
  typedef float f32x2_ __attribute__((ext_vector_type(2)));
  return __builtin_bit_cast(float, __builtin_convertvector(f32x2_{0.f, x}, bf16x2));
}
__device__ __forceinline__ float fold32(float a, float b) {
  auto r = __builtin_amdgcn_permlane32_swap(__float_as_uint(a), __float_as_uint(b), false, false);
  return __uint_as_float(r[0]) + __uint_as_float(r[1]);
}
__device__ __forceinline__ float fold16(float a, float b) {
  auto r = __builtin_amdgcn_permlane16_swap(__float_as_uint(a), __float_as_uint(b), false, false);
  return __uint_as_float(r[0]) + __uint_as_float(r[1]);
}
__device__ __forceinline__ float fold8(float a, float b, bool bit3) {
  const float keep = bit3 ? b : a, send = bit3 ? a : b;
  return keep + dpp<ROR8>(send);
}
__device__ __forceinline__ float max3216(float x) {
  auto s = __builtin_amdgcn_permlane16_swap(__float_as_uint(x), __float_as_uint(x), false, false);
  x = fmaxf(__uint_as_float(s[0]), __uint_as_float(s[1]));
  auto t = __builtin_amdgcn_permlane32_swap(__float_as_uint(x), __float_as_uint(x), false, false);
  return fmaxf(__uint_as_float(t[0]), __uint_as_float(t[1]));
}
__device__ __forceinline__ float row_sum16(float x) {
  x += dpp<XOR1>(x); x += dpp<XOR2>(x); x += dpp<HMIR>(x); x += dpp<ROR8>(x);
  return x;
}
__device__ __forceinline__ unsigned long long mix64(unsigned long long z) {
  z += 0x9E3779B97F4A7C15ull;
  z = (z ^ (z >> 30)) * 0xBF58476D1CE4E5B9ull;
  z = (z ^ (z >> 27)) * 0x94D049BB133111EBull;
  return z ^ (z >> 31);
}
__device__ __forceinline__ u32x4 pack2(float a, float b, unsigned t8, unsigned thi) {
  u32x4 r; r.x = (__float_as_uint(a) >> 8) | t8; r.y = thi; r.z = (__float_as_uint(b) >> 8) | t8; r.w = thi; return r;
}
__device__ __forceinline__ unsigned tag_bad(u32x4 g, unsigned t8, unsigned thi) {
  return (((g.x ^ t8) | (g.z ^ t8)) >> 24) | (g.y ^ thi) | (g.w ^ thi);
}
__device__ __forceinline__ float g0(u32x4 g) { return __uint_as_float(g.x << 8); }
__device__ __forceinline__ float g1(u32x4 g) { return __uint_as_float(g.z << 8); }
__device__ __forceinline__ f32x2 g01(u32x4 g) { return f32x2{g0(g), g1(g)}; }
__device__ __forceinline__ unsigned vbit(float x) { return __float_as_uint(x) | 1u; }
__device__ __forceinline__ f32x2 asf2(unsigned a, unsigned b) { return f32x2{__uint_as_float(a), __uint_as_float(b)}; }
__device__ __forceinline__ float ln_den_inv(float var) {
  return __builtin_amdgcn_rcpf(rbf(__builtin_amdgcn_sqrtf(rbf(rbf(var) + 1e-5f))));
}
__device__ __forceinline__ float rdlane(float x, int l) { return __uint_as_float(__builtin_amdgcn_readlane(__float_as_uint(x), l)); }
__device__ __forceinline__ f32x2 splat(float x) { return f32x2{x, x}; }
__device__ __forceinline__ f32x2 fma2(f32x2 a, f32x2 b, f32x2 c) { return __builtin_elementwise_fma(a, b, c); }

#define SB() __builtin_amdgcn_sched_barrier(0)
#define MEMBAR() asm volatile("" ::: "memory")
#define LD128(rs, vo, so, aux) __builtin_amdgcn_raw_buffer_load_b128((rs), (vo), (so), (aux))
#define ST128(v, rs, vo, so) __builtin_amdgcn_raw_buffer_store_b128((v), (rs), (vo) + (so), 0, AUX_SC1)
#define LD64(rs, vo, so, aux) __builtin_amdgcn_raw_buffer_load_b64((rs), (vo), (so), (aux))
#define ST64(v, rs, vo, so) __builtin_amdgcn_raw_buffer_store_b64((v), (rs), (vo), (so), AUX_SC1)
#define WAIT_VM3() __builtin_amdgcn_s_waitcnt(0x0F73)
#define STAMP(i) do { } while (0)

constexpr unsigned SPIN_CAP = 1u << 17;

__global__ void __launch_bounds__(NTHR, 2)
fused_kernel(const char* x, const char* kc, const char* vc, const char* wq, const char* wo, const char* w1,
             const char* b1, const char* w2, const char* b2, char* out, char* ws) {
  __shared__ __attribute__((aligned(16))) float lds[L_TOTAL];
  unsigned* const ldu = reinterpret_cast<unsigned*>(lds);
  const int tid = threadIdx.x, lane = tid & 63, wave = __builtin_amdgcn_readfirstlane(tid >> 6);
  const int bid = blockIdx.x, team = bid & 7, jj = bid >> 3, hl = jj >> 3, b = (jj >> 2) & 1, sp = jj & 3;
  const int h = team * 4 + hl, cb = hl * 4 + sp, pq = jj & 7;
  const int r = lane >> 3, c = lane & 7;
  const bool bit3 = (lane & 8) != 0;
  const int fidx = 8 * c + 4 * (lane >> 5) + 2 * ((lane >> 4) & 1) + (bit3 ? 1 : 0);
  STAMP(0);
  asm volatile("" : "+s"(x), "+s"(kc), "+s"(vc), "+s"(wq), "+s"(wo), "+s"(w1), "+s"(b1), "+s"(w2), "+s"(b2), "+s"(out), "+s"(ws));
  const __amdgpu_buffer_rsrc_t rw = __builtin_amdgcn_make_buffer_rsrc((void*)ws, 0, WS_END, RSRC3);
  const __amdgpu_buffer_rsrc_t rx = __builtin_amdgcn_make_buffer_rsrc((void*)x, 0, 2 * D * 2, RSRC3);
  constexpr float SCALE = 0.07216878364870322f, LOG2E = 1.4426950408889634f;
  unsigned vzero;
  asm volatile("v_mov_b32 %0, 0" : "=v"(vzero));

  f32x2 fw1, fb1, fw2; float gw1, gb1, gw2, fb2;
  unsigned xres;
  unsigned short a1[3], a2[3], a3[3], a4;
#define LOAD_SMALL() { \
    const __amdgpu_buffer_rsrc_t r1 = __builtin_amdgcn_make_buffer_rsrc((void*)w1, 0, FF * 2 + 4, RSRC3); \
    const __amdgpu_buffer_rsrc_t r2 = __builtin_amdgcn_make_buffer_rsrc((void*)b1, 0, FF * 2 + 4, RSRC3); \
    const __amdgpu_buffer_rsrc_t r3 = __builtin_amdgcn_make_buffer_rsrc((void*)w2, 0, FF * 2 + 4, RSRC3); \
    _Pragma("unroll") for (int k = 0; k < 3; ++k) { \
      a1[k] = __builtin_amdgcn_raw_buffer_load_b16(r1, tid * 2 + 1024 * k, 0, 0); \
      a2[k] = __builtin_amdgcn_raw_buffer_load_b16(r2, tid * 2 + 1024 * k, 0, 0); \
      a3[k] = __builtin_amdgcn_raw_buffer_load_b16(r3, tid * 2 + 1024 * k, 0, 0); \
    } \
    a4 = __builtin_amdgcn_raw_buffer_load_b16(__builtin_amdgcn_make_buffer_rsrc((void*)b2, 0, 4, RSRC3), vzero, 0, 0); \
    xres = __builtin_amdgcn_raw_buffer_load_b32(rx, tid >= 192 ? (tid - 192) * 4 : OOB, b * D * 2, 0); \
    MEMBAR(); }
#define CONV_SMALL() { \
    fw1 = f32x2{bflo(a1[0]), bflo(a1[1])}; fb1 = f32x2{bflo(a2[0]), bflo(a2[1])}; fw2 = f32x2{bflo(a3[0]), bflo(a3[1])}; \
    gw1 = bflo(a1[2]); gb1 = bflo(a2[2]); gw2 = bflo(a3[2]); fb2 = bflo(a4); \
    asm volatile("" : "+v"(fw1), "+v"(fb1), "+v"(fw2), "+v"(gw1), "+v"(gb1), "+v"(gw2), "+v"(fb2), "+v"(xres) :: "memory"); }

  u32x4 wop[6][2];
  unsigned* const wl = ldu + L_WO + ((wave & 3) * 12 * 64 + lane) * 4;
  if (wave >= NH) {
    const int sw = wave - NH;
    const size_t kvbase = (size_t)(b * H + h) * HEAD_BYTES;
    const __amdgpu_buffer_rsrc_t rk = __builtin_amdgcn_make_buffer_rsrc((void*)(kc + kvbase), 0, HEAD_BYTES, RSRC3);
    const __amdgpu_buffer_rsrc_t rv = __builtin_amdgcn_make_buffer_rsrc((void*)(vc + kvbase), 0, HEAD_BYTES, RSRC3);
    const unsigned kvo = r * (DH * 2) + c * 16;
    u32x4 buf[RPRE][3];
    const unsigned so0 = (sp * NSW + sw) * TILE_BYTES + (h & 1) * RA * STRIDE;
    unsigned so = so0;
    static_assert(RA + RPRE + 2 < STEPS && RPRE % 2 == 0 && RPRE >= 2, "no wrap before the main loop");
#pragma unroll
    for (int j = 0; j < RPRE; ++j) {
      if (j >= 2) { WAIT_VM3(); SB(); }
#pragma unroll
      for (int t = 0; t < 3; ++t) { buf[j][t] = LD128(rk, kvo + t * 128, so, AUX_NT); SB(); }
      so += STRIDE;
    }
    LOAD_SMALL();
    STAMP(8);
    __syncthreads();
    u32x4 q[3];
#pragma unroll
    for (int t = 0; t < 3; ++t) q[t] = *reinterpret_cast<const u32x4*>(ldu + L_Q + (8 * t + c) * 4);
    STAMP(1);
    float* sc = lds + L_SC + sw * 512 + r;

#define PIN12(A) asm volatile("" : "+v"(A[0]), "+v"(A[1]), "+v"(A[2]), "+v"(A[3]), "+v"(A[4]), "+v"(A[5]), "+v"(A[6]), "+v"(A[7]), \
    "+v"(A[8]), "+v"(A[9]), "+v"(A[10]), "+v"(A[11]) :: "memory")
#define ADVANCE() { so += STRIDE; if (so >= HEAD_BYTES) so -= HEAD_BYTES; }
#define KSTEP(B, REFILL, RS, SLOT) { \
    float s0_ = 0.f, s1_ = 0.f, s2_ = 0.f; \
    _Pragma("unroll") for (int j_ = 0; j_ < 4; ++j_) { s0_ = dot2(B[0][j_], q[0][j_], s0_); s1_ = dot2(B[1][j_], q[1][j_], s1_); s2_ = dot2(B[2][j_], q[2][j_], s2_); } \
    asm volatile("" : "+v"(s0_), "+v"(s1_), "+v"(s2_) :: "memory"); \
    SB(); \
    if (REFILL) { _Pragma("unroll") for (int u_ = 0; u_ < 3; ++u_) B[u_] = LD128(RS, kvo + u_ * 128, so, AUX_NT); ADVANCE(); } \
    SB(); \
    float s_ = s0_ + s1_ + s2_; \
    s_ += dpp<XOR1>(s_); s_ += dpp<XOR2>(s_); s_ += dpp<HMIR>(s_); \
    s_ = rbf(rbf(s_) * SCALE); \
    if (c == 0) sc[(SLOT) * 8] = s_; \
    MEMBAR(); \
    SB(); }
#define VSTEP(B, REFILL, P) { \
    f32x2 vf_[12]; \
    _Pragma("unroll") for (int t_ = 0; t_ < 3; ++t_) \
      _Pragma("unroll") for (int j_ = 0; j_ < 4; ++j_) vf_[t_ * 4 + j_] = bf2(B[t_][j_]); \
    PIN12(vf_); \
    SB(); \
    if (REFILL) { _Pragma("unroll") for (int u_ = 0; u_ < 3; ++u_) B[u_] = LD128(rv, kvo + u_ * 128, so, AUX_NT); ADVANCE(); } \
    SB(); \
    _Pragma("unroll") for (int k_ = 0; k_ < 12; ++k_) acc[k_] = fma2(splat(P), vf_[k_], acc[k_]); \
    PIN12(acc); \
    SB(); }

    KSTEP(buf[0], 1, rk, 0);
    KSTEP(buf[1], 1, rk, 1);
#pragma unroll
    for (int jb = 2; jb < RPRE; ++jb) KSTEP(buf[jb], 0, rk, jb);
    sc += RPRE * 8;
#pragma nounroll
    for (int i = RPRE; i < STEPS - 2; i += 2) {
      KSTEP(buf[0], 1, rk, 0);
      KSTEP(buf[1], 1, rk, 1);
      sc += 16;
    }
    so = so0;
    KSTEP(buf[0], 1, rv, 0);
    KSTEP(buf[1], 1, rv, 1);
    STAMP(10);
    float m, l;
    {
      f32x4* const sp4 = reinterpret_cast<f32x4*>(lds + L_SC + sw * 512) + lane;
      f32x4 sa = sp4[0], sb = sp4[64];
      float mx = fmaxf(fmaxf(fmaxf(sa.x, sa.y), fmaxf(sa.z, sa.w)), fmaxf(fmaxf(sb.x, sb.y), fmaxf(sb.z, sb.w)));
      mx = fmaxf(mx, dpp<XOR1>(mx)); mx = fmaxf(mx, dpp<XOR2>(mx)); mx = fmaxf(mx, dpp<HMIR>(mx)); mx = fmaxf(mx, dpp<ROR8>(mx));
      mx = max3216(mx);
      m = mx * LOG2E;
#pragma unroll
      for (int k = 0; k < 4; ++k) {
        sa[k] = __builtin_amdgcn_exp2f((sa[k] - mx) * LOG2E);
        sb[k] = __builtin_amdgcn_exp2f((sb[k] - mx) * LOG2E);
      }
      sp4[0] = sa; sp4[64] = sb;
      l = row_sum16(((sa.x + sa.y) + (sa.z + sa.w)) + ((sb.x + sb.y) + (sb.z + sb.w)));
      l = fold16(l, l); l = fold32(l, l);
      if (lane == 0) *reinterpret_cast<f32x2*>(lds + L_ML + 2 * sw) = f32x2{m, l};
      MEMBAR();
    }
    f32x2 acc[12];
#pragma unroll
    for (int k = 0; k < 12; ++k) acc[k] = f32x2{0.f, 0.f};
    sc = lds + L_SC + sw * 512 + r;
#pragma nounroll
    for (int i = 0; i < STEPS - 2; i += 2) {
      const float p0 = sc[0], p1 = sc[8];
      sc += 16;
      SB();
      VSTEP(buf[0], 1, p0);
      VSTEP(buf[1], 1, p1);
    }
    {
      const float p0 = sc[0], p1 = sc[8];
      VSTEP(buf[0], 0, p0);
      VSTEP(buf[1], 0, p1);
    }
    STAMP(2);
    {
      float* const red = lds + L_RED + sw * DH + fidx;
#pragma unroll
      for (int t = 0; t < 3; ++t) {
        const float z0 = fold32(acc[t * 4 + 0].x, acc[t * 4 + 2].x), z1 = fold32(acc[t * 4 + 0].y, acc[t * 4 + 2].y);
        const float z2 = fold32(acc[t * 4 + 1].x, acc[t * 4 + 3].x), z3 = fold32(acc[t * 4 + 1].y, acc[t * 4 + 3].y);
        red[64 * t] = fold8(fold16(z0, z2), fold16(z1, z3), bit3);
      }
      unsigned arrived = 0;
      if (lane == 0) arrived = __hip_atomic_fetch_add(ldu + L_CNT, 1u, __ATOMIC_ACQ_REL, __HIP_MEMORY_SCOPE_WORKGROUP);
      if (__builtin_amdgcn_readfirstlane(arrived) == NSW - 1) {
        const int d4 = lane < 48 ? 4 * lane : 0;
        const f32x4 ml01 = *reinterpret_cast<const f32x4*>(lds + L_ML), ml23 = *reinterpret_cast<const f32x4*>(lds + L_ML + 4);
        f32x4 v[NSW];
#pragma unroll
        for (int w = 0; w < NSW; ++w) v[w] = *reinterpret_cast<const f32x4*>(lds + L_RED + w * DH + d4);
        const float M = __uint_as_float(vbit(fmaxf(fmaxf(ml01.x, ml01.z), fmaxf(ml23.x, ml23.z))));
        const float f0 = __builtin_amdgcn_exp2f(ml01.x - M), f1 = __builtin_amdgcn_exp2f(ml01.z - M);
        const float f2 = __builtin_amdgcn_exp2f(ml23.x - M), f3 = __builtin_amdgcn_exp2f(ml23.z - M);
        f32x4 o = (v[0] * f0 + v[1] * f1) + (v[2] * f2 + v[3] * f3);
        const float Ls = (ml01.y * f0 + ml01.w * f1) + (ml23.y * f2 + ml23.w * f3);
        if (lane >= 48) { o.x = M; o.y = Ls; }
        ST128((u32x4{vbit(o.x), vbit(o.y), vbit(o.z), vbit(o.w)}), rw, lane <= 48 ? lane * 16 : OOB, WS_P + bid * 1024);
      }
    }
    STAMP(3);
  } else {
    if (wave < 3 && !NOQ) {
      const __amdgpu_buffer_rsrc_t rq = __builtin_amdgcn_make_buffer_rsrc((void*)wq, 0, D * H * DH * 2, RSRC3);
      unsigned xs[2][5], xp[2];
#pragma unroll
      for (int bb = 0; bb < 2; ++bb) {
#pragma unroll
        for (int i = 0; i < 5; ++i) xs[bb][i] = __builtin_amdgcn_raw_buffer_load_b32(rx, lane * 4 + i * 256 + bb * D * 2, 0, 0);
        xp[bb] = __builtin_amdgcn_raw_buffer_load_b32(rx, (c < 5 ? (8 * c + r) * 4 : 0) + bb * D * 2, pq * 160, 0);
      }
      const u32x2 ep = __builtin_amdgcn_raw_buffer_load_b64(rw, vzero, WS_EPOCH, AUX_SC1);
      SB();
      u32x4 wqr[5][2];
      {
        const unsigned vo = (unsigned)(2 * r) * (H * DH * 2) + c * 16;
        const unsigned qso = (unsigned)(80 * pq) * (H * DH * 2) + h * (DH * 2) + wave * 128;
#pragma unroll
        for (int i = 0; i < 5; ++i)
#pragma unroll
          for (int e = 0; e < 2; ++e) { wqr[i][e] = LD128(rq, vo, qso + (16 * i + e) * (H * DH * 2), AUX_NT); SB(); }
      }
      float s0 = 0.f, s1 = 0.f, q0 = 0.f, q1 = 0.f;
#pragma unroll
      for (int i = 0; i < 5; ++i) {
        s0 = dot2(xs[0][i], ONES, s0); q0 = dot2(xs[0][i], xs[0][i], q0);
        s1 = dot2(xs[1][i], ONES, s1); q1 = dot2(xs[1][i], xs[1][i], q1);
      }
      const float st = row_sum16(fold16(fold32(s0, s1), fold32(q0, q1)));
      auto sw2 = __builtin_amdgcn_permlane16_swap(__float_as_uint(st), __float_as_uint(st), false, false);
      const float mean = __uint_as_float(sw2[0]) * (1.0f / D);
      const float var = fmaxf(fmaf(-mean, mean, __uint_as_float(sw2[1]) * (1.0f / D)), 0.f);
      const float inv = ln_den_inv(var), mb = rbf(mean);
      const float m0 = rdlane(mb, 0), m1 = rdlane(mb, 32), i0 = rdlane(inv, 0), i1 = rdlane(inv, 32);
      const unsigned xn0 = pack_bf16(rbf(bflo(xp[0]) - m0) * i0, rbf(bfhi(xp[0]) - m0) * i0);
      const unsigned xn1 = pack_bf16(rbf(bflo(xp[1]) - m1) * i1, rbf(bfhi(xp[1]) - m1) * i1);
      const unsigned long long tg = mix64(((unsigned long long)(unsigned)__builtin_amdgcn_readfirstlane(ep.y) << 32) | (unsigned)__builtin_amdgcn_readfirstlane(ep.x));
      const unsigned thi = (unsigned)(tg >> 32), t8 = (unsigned)tg & 0xff000000u;
      float a0[8], a1q[8];
#pragma unroll
      for (int k = 0; k < 8; ++k) { a0[k] = 0.f; a1q[k] = 0.f; }
#define QGROUP(I) { \
        unsigned b0_ = __builtin_amdgcn_update_dpp(0u, xn0, 0x150 + I, 0xf, 0x3, false); b0_ = __builtin_amdgcn_update_dpp(b0_, xn0, 0x158 + I, 0xf, 0xc, false); \
        unsigned b1_ = __builtin_amdgcn_update_dpp(0u, xn1, 0x150 + I, 0xf, 0x3, false); b1_ = __builtin_amdgcn_update_dpp(b1_, xn1, 0x158 + I, 0xf, 0xc, false); \
        _Pragma("unroll") for (int j = 0; j < 4; ++j) { \
          const unsigned plo = __builtin_amdgcn_perm(wqr[I][1][j], wqr[I][0][j], 0x05040100u), phi = __builtin_amdgcn_perm(wqr[I][1][j], wqr[I][0][j], 0x07060302u); \
          a0[2 * j] = dot2(plo, b0_, a0[2 * j]); a0[2 * j + 1] = dot2(phi, b0_, a0[2 * j + 1]); \
          a1q[2 * j] = dot2(plo, b1_, a1q[2 * j]); a1q[2 * j + 1] = dot2(phi, b1_, a1q[2 * j + 1]); } }
      QGROUP(0) QGROUP(1) QGROUP(2) QGROUP(3) QGROUP(4)
      float z[8];
#pragma unroll
      for (int k = 0; k < 8; ++k) z[k] = fold32(a0[k], a1q[k]);
      float y[4];
#pragma unroll
      for (int k = 0; k < 4; ++k) y[k] = fold16(z[k], z[k + 4]);
      const float u0 = fold8(y[0], y[2], bit3), u1 = fold8(y[1], y[3], bit3);
      const unsigned pidx = 32 * wave + 4 * c + 2 * ((lane >> 4) & 1) + (bit3 ? 1 : 0);
      ST128(pack2(u0, u1, t8, thi), rw, ((lane >> 5) * 8 * 96 + pidx) * 16, WS_QP + (h * 16 + pq) * (96 * 16));
      STAMP(8);
      asm volatile("s_waitcnt vmcnt(0)" ::: "memory");
      {
        const unsigned qo = ((lane >> 5) * 96 + 32 * wave + (lane & 31)) * 16;
        const unsigned qb = WS_QP + (h * 16 + b * 8) * (96 * 16);
        u32x4 gq[4];
        for (unsigned spins = 0;; ++spins) {
          MEMBAR();
#pragma unroll
          for (int k = 0; k < 4; ++k) gq[k] = LD128(rw, qo, qb + k * (2 * 96 * 16), AUX_SC1);
          unsigned bad = 0;
#pragma unroll
          for (int k = 0; k < 4; ++k) bad |= tag_bad(gq[k], t8, thi);
          if (__all(bad == 0u) || spins > SPIN_CAP) break;
        }
        const f32x2 qq = (g01(gq[0]) + g01(gq[1])) + (g01(gq[2]) + g01(gq[3]));
        const float qa = fold32(qq.x, qq.x), qc = fold32(qq.y, qq.y);
        if (lane < 32) ldu[L_Q + 32 * wave + lane] = pack_bf16(qa, qc);
      }
      if (tid == 0) *reinterpret_cast<u32x2*>(ldu + L_TAG) = u32x2{(unsigned)tg, thi};
    }
    const unsigned yoff = ((lane >= 20 ? 8 * 320 : 0) + (lane >= 20 ? lane - 20 : lane)) * 16;
    const unsigned ybo = WS_Y + (team * 320 + cb * 20) * 16 + b * 8;
    if (wave == 3) {
      if (lane == 0) ldu[L_CNT] = 0u;
      const u32x2 ep = LD64(rw, vzero, WS_EPOCH, AUX_SC1);
      ST128((u32x4{0u, 0u, 0u, 0u}), rw, lane * 16, WS_P + bid * 1024);
      ST64((u32x2{0u, 0u}), rw, lane < 40 ? yoff : OOB, ybo);
      asm volatile("s_waitcnt vmcnt(0)" ::: "memory");
      const unsigned long long tg = mix64(((unsigned long long)(unsigned)__builtin_amdgcn_readfirstlane(ep.y) << 32) | (unsigned)__builtin_amdgcn_readfirstlane(ep.x));
      ST64((u32x2{(unsigned)tg, (unsigned)(tg >> 32)}), rw, lane == 0 ? 0u : OOB, WS_RDY + (team * 32 + jj) * 8);
    }
    if (NOQ && wave == 0) {
      const u32x2 ep = __builtin_amdgcn_raw_buffer_load_b64(rw, vzero, WS_EPOCH, AUX_SC1);
      const unsigned long long tg = mix64(((unsigned long long)(unsigned)__builtin_amdgcn_readfirstlane(ep.y) << 32) | (unsigned)__builtin_amdgcn_readfirstlane(ep.x));
      if (tid == 0) *reinterpret_cast<u32x2*>(ldu + L_TAG) = u32x2{(unsigned)tg, (unsigned)(tg >> 32)};
      ldu[L_Q + lane] = 0x3c003c00; ldu[L_Q + 32 + lane] = 0x3c003c00;
    }
    STAMP(9);
    __syncthreads();
    STAMP(1);
    LOAD_SMALL();
    const __amdgpu_buffer_rsrc_t ro = __builtin_amdgcn_make_buffer_rsrc((void*)wo, 0, D * H * DH * 2, RSRC3);
    const unsigned vo = c < 5 ? (unsigned)(2 * r) * (D * 2) + c * 16 : OOB;
    const unsigned wso = NOWO ? OOB : (unsigned)(768 * team + 384 * b + 96 * wave) * (D * 2) + cb * 80;
#pragma unroll
    for (int i = 0; i < 6; ++i) {
      const u32x4 e0 = LD128(ro, vo, wso + 16 * i * (D * 2), AUX_NT);
      const u32x4 e1 = LD128(ro, vo + D * 2, wso + 16 * i * (D * 2), AUX_NT);
      u32x4 p0, p1;
      p0.x = __builtin_amdgcn_perm(e1.x, e0.x, 0x05040100u); p0.y = __builtin_amdgcn_perm(e1.x, e0.x, 0x07060302u);
      p0.z = __builtin_amdgcn_perm(e1.y, e0.y, 0x05040100u); p0.w = __builtin_amdgcn_perm(e1.y, e0.y, 0x07060302u);
      p1.x = __builtin_amdgcn_perm(e1.z, e0.z, 0x05040100u); p1.y = __builtin_amdgcn_perm(e1.z, e0.z, 0x07060302u);
      p1.z = __builtin_amdgcn_perm(e1.w, e0.w, 0x05040100u); p1.w = __builtin_amdgcn_perm(e1.w, e0.w, 0x07060302u);
      wop[i][0] = p0; wop[i][1] = p1;
      *reinterpret_cast<u32x4*>(wl + (2 * i) * 256) = p0;
      *reinterpret_cast<u32x4*>(wl + (2 * i + 1) * 256) = p1;
      MEMBAR();
      SB();
    }
    if (wave == 0) {
      const u32x2 tg = *reinterpret_cast<const u32x2*>(ldu + L_TAG);
      for (unsigned spins = 0; spins < SPIN_CAP; ++spins) {
        MEMBAR();
        const u32x4 r0 = LD128(rw, lane * 16, WS_RDY, AUX_SC1), r1 = LD128(rw, lane * 16 + 1024, WS_RDY, AUX_SC1);
        const unsigned bad = (r0.x ^ tg.x) | (r0.y ^ tg.y) | (r0.z ^ tg.x) | (r0.w ^ tg.y) | (r1.x ^ tg.x) | (r1.y ^ tg.y) | (r1.z ^ tg.x) | (r1.w ^ tg.y);
        if (__all(bad == 0u)) break;
        __builtin_amdgcn_s_sleep(32);
      }
    }
  }
  const u32x2 tg2 = *reinterpret_cast<const u32x2*>(ldu + L_TAG);
  const unsigned tlo = __builtin_amdgcn_readfirstlane(tg2.x), thi = __builtin_amdgcn_readfirstlane(tg2.y);

  __syncthreads();
  {
    const unsigned pbase = WS_P + (unsigned)(team + 8 * ((2 * b + ((wave >> 1) & 1)) * 8 + (wave >> 2) * 4)) * 1024;
    const unsigned po = (lane < 48 ? (wave & 1) * 48 + lane : 96) * 8;
    u32x2 go[GS];
    MEMBAR();
#pragma unroll
    for (int s = 0; s < GS; ++s) go[s] = LD64(rw, po, pbase + s * (8 * 1024), AUX_SC1);
    MEMBAR();
    SB();
    if (wave >= NH) {
#pragma unroll
      for (int i = 0; i < 6; ++i) {
        wop[i][0] = *reinterpret_cast<const u32x4*>(wl + (2 * i) * 256);
        wop[i][1] = *reinterpret_cast<const u32x4*>(wl + (2 * i + 1) * 256);
      }
    }
    MEMBAR();
    unsigned pend = 0, spins = 0;
#pragma unroll
    for (int s = 0; s < GS; ++s)
      if (!__all((go[s].x & go[s].y & 1u) != 0u)) pend |= 1u << s;
    while (pend != 0u && ++spins < SPIN_CAP) {
      MEMBAR();
#pragma unroll
      for (int s = 0; s < GS; ++s)
        if (pend & (1u << s)) go[s] = LD64(rw, po, pbase + s * (8 * 1024), AUX_SC1);
#pragma unroll
      for (int s = 0; s < GS; ++s)
        if (pend & (1u << s)) { if (__all((go[s].x & go[s].y & 1u) != 0u)) pend &= ~(1u << s); }
    }
    const int sl = lane & 3;
    const u32x2 gm = sl == 0 ? go[0] : sl == 1 ? go[1] : sl == 2 ? go[2] : go[3];
    const float ms = __uint_as_float(gm.x);
    float M = fmaxf(ms, dpp<XOR1>(ms)); M = fmaxf(M, dpp<XOR2>(M));
    const float f = __builtin_amdgcn_exp2f(ms - M);
    float Ls = f * __uint_as_float(gm.y);
    Ls += dpp<XOR1>(Ls); Ls += dpp<XOR2>(Ls);
    const float wgt = f * __builtin_amdgcn_rcpf(Ls);
    f32x2 oa = splat(rdlane(wgt, 48)) * asf2(go[0].x, go[0].y), ob = splat(rdlane(wgt, 49)) * asf2(go[1].x, go[1].y);
    oa = fma2(splat(rdlane(wgt, 50)), asf2(go[2].x, go[2].y), oa); ob = fma2(splat(rdlane(wgt, 51)), asf2(go[3].x, go[3].y), ob);
    oa += ob;
    if (lane < 48) ldu[L_O + 48 * wave + lane] = pack_bf16(oa.x, oa.y);
  }
  STAMP(4);
  {
    unsigned op[6];
#pragma unroll
    for (int i = 0; i < 6; ++i) op[i] = ldu[L_O + 48 * wave + 8 * i + r];
    float y[8];
#pragma unroll
    for (int k = 0; k < 8; ++k) y[k] = 0.f;
#pragma unroll
    for (int i = 0; i < 6; ++i) {
#pragma unroll
      for (int k = 0; k < 4; ++k) { y[k] = dot2(wop[i][0][k], op[i], y[k]); y[k + 4] = dot2(wop[i][1][k], op[i], y[k + 4]); }
    }
    const float z0 = fold32(y[0], y[4]), z1 = fold32(y[1], y[5]), z2 = fold32(y[2], y[6]), z3 = fold32(y[3], y[7]);
    const float u = fold8(fold16(z0, z2), fold16(z1, z3), bit3);
    if (c < 5) lds[L_YR + wave * 40 + fidx] = u;
  }
  __syncthreads();
  if (tid < 40) {
    const int bb = tid >= 20, t = tid - 20 * bb;
    f32x2 v[4];
#pragma unroll
    for (int w = 0; w < 4; ++w) v[w] = *reinterpret_cast<const f32x2*>(lds + L_YR + (4 * bb + w) * 40 + 2 * t);
    const f32x2 ys = (v[0] + v[1]) + (v[2] + v[3]);
    ST64((u32x2{vbit(ys.x), vbit(ys.y)}), rw, (bb * 8 * 320 + t) * 16, WS_Y + (team * 320 + cb * 20) * 16 + b * 8);
  }
  STAMP(5);
  __syncthreads();
  CONV_SMALL();
  if (wave >= 3) {
    u32x4 gy[8];
    const unsigned yo = (tid - 192) * 16, ybase = WS_Y + b * (8 * 320 * 16);
    unsigned pend = 0xffu, spins = 0;
    do {
      MEMBAR();
#pragma unroll
      for (int s = 0; s < 8; ++s)
        if (pend & (1u << s)) gy[s] = LD128(rw, yo, ybase + s * (320 * 16), AUX_SC1);
#pragma unroll
      for (int s = 0; s < 8; ++s)
        if (pend & (1u << s)) { if (__all((gy[s].x & gy[s].y & gy[s].z & gy[s].w & 1u) != 0u)) pend &= ~(1u << s); }
    } while (pend != 0u && ++spins < SPIN_CAP);
    f32x2 ap[8];
#pragma unroll
    for (int s = 0; s < 8; ++s) ap[s] = asf2(gy[s].x, gy[s].y) + asf2(gy[s].z, gy[s].w);
    const f32x2 a = ((ap[0] + ap[1]) + (ap[2] + ap[3])) + ((ap[4] + ap[5]) + (ap[6] + ap[7]));
    const unsigned hp = pack_bf16(bflo(xres) + rbf(a.x), bfhi(xres) + rbf(a.y));
    ldu[L_H + tid - 192] = hp;
    float st = fold32(dot2(hp, ONES, 0.f), dot2(hp, hp, 0.f));
    st = row_sum16(fold16(st, st));
    if ((lane & 31) == 0) lds[L_ST + (wave - 3) * 2 + (lane >> 5)] = st;
  }
  __syncthreads();
  STAMP(6);
  const int d0 = 5 * (team * 16 + cb);
  float he;
  {
    const int de = d0 + (lane < 5 ? lane : 0);
    const unsigned hw = ldu[L_H + (de >> 1)];
    f32x2 sv[5];
#pragma unroll
    for (int w = 0; w < 5; ++w) sv[w] = *reinterpret_cast<const f32x2*>(lds + L_ST + 2 * w);
    const f32x2 st = (((sv[0] + sv[1]) + (sv[2] + sv[3])) + sv[4]) * splat(1.0f / D);
    he = (de & 1) ? bfhi(hw) : bflo(hw);
    const float mean1 = st.x;
    const float var1 = fmaxf(fmaf(-mean1, mean1, st.y), 0.f);
    const float i1 = ln_den_inv(var1), m1 = rbf(mean1);
    const float m2 = rbf((mean1 - m1) * i1), i2 = ln_den_inv(var1 * i1 * i1);
    const float tl = rbf(rbf(rbf(rbf(he - m1) * i1) - m2) * i2);
    constexpr float G1 = -2.0f * 0.7978845608028654f * LOG2E, G3 = G1 * 0.044715f;
    float te[5];
#pragma unroll
    for (int e = 0; e < 5; ++e) te[e] = rdlane(tl, e);
    f32x2 u[8], z[8];
#pragma unroll
    for (int e = 0; e < 5; ++e) u[e] = fma2(splat(te[e]), fw1, fb1);
    u[5] = fma2(f32x2{te[0], te[1]}, splat(gw1), splat(gb1));
    u[6] = fma2(f32x2{te[2], te[3]}, splat(gw1), splat(gb1));
    u[7] = fma2(splat(te[4]), splat(gw1), splat(gb1));
    SB();
#pragma unroll
    for (int k = 0; k < 8; ++k) z[k] = u[k] * u[k];
    SB();
#pragma unroll
    for (int k = 0; k < 8; ++k) z[k] = fma2(z[k], splat(G3), splat(G1));
    SB();
#pragma unroll
    for (int k = 0; k < 8; ++k) z[k] = z[k] * u[k];
    SB();
#pragma unroll
    for (int k = 0; k < 8; ++k) { z[k].x = __builtin_amdgcn_exp2f(z[k].x); if (k < 7) z[k].y = __builtin_amdgcn_exp2f(z[k].y); }
#pragma unroll
    for (int k = 0; k < 8; ++k) u[k] = u[k] * (k < 5 ? fw2 : splat(gw2));
    SB();
#pragma unroll
    for (int k = 0; k < 8; ++k) z[k] = z[k] + splat(1.0f);
    SB();
#pragma unroll
    for (int k = 0; k < 8; ++k) { z[k].x = __builtin_amdgcn_rcpf(z[k].x); if (k < 7) z[k].y = __builtin_amdgcn_rcpf(z[k].y); }
    SB();
#pragma unroll
    for (int k = 0; k < 8; ++k) u[k] = u[k] * z[k];
    const float ya0 = (u[0].x + u[0].y) + u[5].x, ya1 = (u[1].x + u[1].y) + u[5].y, ya2 = (u[2].x + u[2].y) + u[6].x,
                ya3 = (u[3].x + u[3].y) + u[6].y, ya4 = (u[4].x + u[4].y) + u[7].x;
    const float f4 = fold32(ya4, ya4);
    float r0 = fold8(fold16(fold32(ya0, ya1), fold32(ya2, ya3)), fold16(f4, f4), bit3);
    r0 += dpp<XOR1>(r0); r0 += dpp<XOR2>(r0); r0 += dpp<HMIR>(r0);
    const int el = bit3 ? 4 : (lane >> 5) + 2 * ((lane >> 4) & 1);
    if ((lane & 15) == 0 || lane == 8) lds[L_MR + el * 8 + wave] = r0;
  }
  __syncthreads();
  if (tid < 5) {
    const f32x4 pa = *reinterpret_cast<const f32x4*>(lds + L_MR + tid * 8), pb = *reinterpret_cast<const f32x4*>(lds + L_MR + tid * 8 + 4);
    const f32x4 ps = pa + pb;
    const float yv = (ps.x + ps.y) + (ps.z + ps.w);
    const unsigned o = pack_bf16(he + rbf(rbf(yv) + fb2), 0.f);
    __builtin_amdgcn_raw_buffer_store_b16((unsigned short)o, __builtin_amdgcn_make_buffer_rsrc((void*)out, 0, 2 * D * 2 + 4, RSRC3),
                                          tid * 2, (b * D + d0) * 2, 0);
  }
  if (tid == 0 && bid == 0)
    __builtin_amdgcn_raw_buffer_store_b64(u32x2{tlo, thi}, rw, 0, WS_EPOCH, AUX_SC1);
  STAMP(7);
}

}

extern "C" void kernel_launch(void* const* d_in, const int* in_sizes, int n_in, void* d_out, int out_size, void* d_ws,
                              size_t ws_size, hipStream_t stream) {
  (void)in_sizes; (void)n_in; (void)out_size; (void)ws_size;
  fused_kernel<<<dim3(NWG), dim3(NTHR), 0, stream>>>(
      (const char*)d_in[0], (const char*)d_in[1], (const char*)d_in[2], (const char*)d_in[3], (const char*)d_in[4],
      (const char*)d_in[6], (const char*)d_in[7], (const char*)d_in[8], (const char*)d_in[9], (char*)d_out, (char*)d_ws);
}
```

```cpp
#include <hip/hip_runtime.h>
#include <hip/hip_bf16.h>
#include <stdint.h>

namespace {

typedef unsigned u32x4 __attribute__((ext_vector_type(4)));
typedef unsigned u32x2 __attribute__((ext_vector_type(2)));
typedef float f32x2 __attribute__((ext_vector_type(2)));
typedef float f32x4 __attribute__((ext_vector_type(4)));
typedef __bf16 bf16x2 __attribute__((ext_vector_type(2)));

constexpr int D = 640, H = 32, SK = 8192, DH = 192, FF = 1536;
constexpr int NWG = 256, NTHR = 512, NWAVE = 8;
constexpr unsigned TILE_BYTES = 8 * DH * 2;
constexpr unsigned HEAD_BYTES = SK * DH * 2;
#define NOQ 0
#define NOWO 0
#define RA 8
#define EDELAY 2
#define WOSL 127
#define WOST 0
#define RPRE 8
constexpr int GS = 8;
constexpr int NSW = 4, NH = NWAVE - NSW;
constexpr int WSTEPS = SK / 8 / (GS * NSW);
constexpr int STEPS = 2 * WSTEPS;
constexpr unsigned STRIDE = GS * NSW * TILE_BYTES;
constexpr int AUX_NT = 2, AUX_SC1 = 16;
constexpr unsigned RSRC3 = 0x00020000u;
constexpr unsigned OOB = 0x80000000u;

constexpr unsigned WS_EPOCH = 0;
constexpr unsigned WS_RDY = 4096;
constexpr unsigned WS_P = 8192;
constexpr unsigned WS_Y = WS_P + NWG * 2048;
constexpr unsigned WS_QP = WS_Y + 2 * 8 * 320 * 16;
constexpr unsigned WS_END = WS_QP + 32 * 2 * 8 * 96 * 16;

constexpr int L_RED = 0;
constexpr int L_ML = L_RED + 2 * NSW * DH;
constexpr int L_Q = L_ML + 16;
constexpr int L_TAG = L_Q + 192;
constexpr int L_CNT = L_TAG + 4;
constexpr int L_O = L_CNT + 8;
constexpr int L_YR = L_O + 384;
constexpr int L_H = L_YR + 512;
constexpr int L_ST = L_H + 320;
constexpr int L_MR = L_ST + 16;
constexpr int L_SC = L_MR + 64;
constexpr int L_WO = L_SC + NSW * 512;
constexpr int L_TOTAL = 24 * 1024;
static_assert(L_WO + 24 * 2 * 64 * 4 <= L_TOTAL, "lds");
static_assert(L_WO % 4 == 0 && L_Q % 4 == 0 && L_ST % 4 == 0 && L_MR % 4 == 0, "alignment");

__device__ __forceinline__ float dot2(unsigned a, unsigned b, float c) {
  return __builtin_amdgcn_fdot2_f32_bf16(__builtin_bit_cast(bf16x2, a), __builtin_bit_cast(bf16x2, b), c, false);
}
template <int CTRL> __device__ __forceinline__ float dpp(float x) {
  return __builtin_bit_cast(float, __builtin_amdgcn_mov_dpp(__builtin_bit_cast(int, x), CTRL, 0xf, 0xf, true));
}
constexpr int XOR1 = 0xB1, XOR2 = 0x4E, HMIR = 0x141, ROR8 = 0x128;
constexpr unsigned ONES = 0x3f803f80u;
__device__ __forceinline__ float bflo(unsigned u) { return __builtin_bit_cast(float, u << 16); }
__device__ __forceinline__ float bfhi(unsigned u) { return __builtin_bit_cast(float, u & 0xffff0000u); }
__device__ __forceinline__ f32x2 bf2(unsigned u) { return f32x2{bflo(u), bfhi(u)}; }
__device__ __forceinline__ unsigned pack_bf16(float lo, float hi) {
  bf16x2 v; v[0] = (__bf16)lo; v[1] = (__bf16)hi;
  return __builtin_bit_cast(unsigned, v);
}
__device__ __forceinline__ float rbf(float x) {
  typedef float f32x2_ __attribute__((ext_vector_type(2)));
  return __builtin_bit_cast(float, __builtin_convertvector(f32x2_{0.f, x}, bf16x2));
}
__device__ __forceinline__ float fold32(float a, float b) {
  auto r = __builtin_amdgcn_permlane32_swap(__float_as_uint(a), __float_as_uint(b), false, false);
  return __uint_as_float(r[0]) + __uint_as_float(r[1]);
}
__device__ __forceinline__ float fold16(float a, float b) {
  auto r = __builtin_amdgcn_permlane16_swap(__float_as_uint(a), __float_as_uint(b), false, false);
  return __uint_as_float(r[0]) + __uint_as_float(r[1]);
}
__device__ __forceinline__ float fold8(float a, float b, bool bit3) {
  const float keep = bit3 ? b : a, send = bit3 ? a : b;
  return keep + dpp<ROR8>(send);
}
__device__ __forceinline__ float max3216(float x) {
  auto s = __builtin_amdgcn_permlane16_swap(__float_as_uint(x), __float_as_uint(x), false, false);
  x = fmaxf(__uint_as_float(s[0]), __uint_as_float(s[1]));
  auto t = __builtin_amdgcn_permlane32_swap(__float_as_uint(x), __float_as_uint(x), false, false);
  return fmaxf(__uint_as_float(t[0]), __uint_as_float(t[1]));
}
__device__ __forceinline__ float row_sum16(float x) {
  x += dpp<XOR1>(x); x += dpp<XOR2>(x); x += dpp<HMIR>(x); x += dpp<ROR8>(x);
  return x;
}
__device__ __forceinline__ unsigned long long mix64(unsigned long long z) {
  z += 0x9E3779B97F4A7C15ull;
  z = (z ^ (z >> 30)) * 0xBF58476D1CE4E5B9ull;
  z = (z ^ (z >> 27)) * 0x94D049BB133111EBull;
  return z ^ (z >> 31);
}
__device__ __forceinline__ u32x4 pack2(float a, float b, unsigned t8, unsigned thi) {
  u32x4 r; r.x = (__float_as_uint(a) >> 8) | t8; r.y = thi; r.z = (__float_as_uint(b) >> 8) | t8; r.w = thi; return r;
}
__device__ __forceinline__ unsigned tag_bad(u32x4 g, unsigned t8, unsigned thi) {
  return (((g.x ^ t8) | (g.z ^ t8)) >> 24) | (g.y ^ thi) | (g.w ^ thi);
}
__device__ __forceinline__ float g0(u32x4 g) { return __uint_as_float(g.x << 8); }
__device__ __forceinline__ float g1(u32x4 g) { return __uint_as_float(g.z << 8); }
__device__ __forceinline__ f32x2 g01(u32x4 g) { return f32x2{g0(g), g1(g)}; }
__device__ __forceinline__ unsigned vbit(float x) { return __float_as_uint(x) | 1u; }
__device__ __forceinline__ f32x2 asf2(unsigned a, unsigned b) { return f32x2{__uint_as_float(a), __uint_as_float(b)}; }
__device__ __forceinline__ float ln_den_inv(float var) {
  return __builtin_amdgcn_rcpf(rbf(__builtin_amdgcn_sqrtf(rbf(rbf(var) + 1e-5f))));
}
__device__ __forceinline__ float rdlane(float x, int l) { return __uint_as_float(__builtin_amdgcn_readlane(__float_as_uint(x), l)); }
__device__ __forceinline__ f32x2 splat(float x) { return f32x2{x, x}; }
__device__ __forceinline__ f32x2 fma2(f32x2 a, f32x2 b, f32x2 c) { return __builtin_elementwise_fma(a, b, c); }

#define SB() __builtin_amdgcn_sched_barrier(0)
#define MEMBAR() asm volatile("" ::: "memory")
#define LD128(rs, vo, so, aux) __builtin_amdgcn_raw_buffer_load_b128((rs), (vo), (so), (aux))
#define ST128(v, rs, vo, so) __builtin_amdgcn_raw_buffer_store_b128((v), (rs), (vo) + (so), 0, AUX_SC1)
#define LD64(rs, vo, so, aux) __builtin_amdgcn_raw_buffer_load_b64((rs), (vo), (so), (aux))
#define ST64(v, rs, vo, so) __builtin_amdgcn_raw_buffer_store_b64((v), (rs), (vo), (so), AUX_SC1)
#define WAIT_VM3() __builtin_amdgcn_s_waitcnt(0x0F73)
#define STAMP(i) do { } while (0)

constexpr unsigned SPIN_CAP = 1u << 17;

__global__ void __launch_bounds__(NTHR, 2)
fused_kernel(const char* x, const char* kc, const char* vc, const char* wq, const char* wo, const char* w1,
             const char* b1, const char* w2, const char* b2, char* out, char* ws) {
  __shared__ __attribute__((aligned(16))) float lds[L_TOTAL];
  unsigned* const ldu = reinterpret_cast<unsigned*>(lds);
  const int tid = threadIdx.x, lane = tid & 63, wave = __builtin_amdgcn_readfirstlane(tid >> 6);
  const int bid = blockIdx.x, team = bid & 7, jj = bid >> 3, hl = jj >> 3, b = (jj >> 2) & 1, sp = jj & 3;
  const int h = team * 4 + hl, cb = hl * 4 + sp, pq = jj & 7;
  const int pc = sp * 4 + hl;
  const bool proj = pc < 10;
  const int r = lane >> 3, c = lane & 7;
  const bool bit3 = (lane & 8) != 0;
  const int fidx = 8 * c + 4 * (lane >> 5) + 2 * ((lane >> 4) & 1) + (bit3 ? 1 : 0);
  STAMP(0);
  asm volatile("" : "+s"(x), "+s"(kc), "+s"(vc), "+s"(wq), "+s"(wo), "+s"(w1), "+s"(b1), "+s"(w2), "+s"(b2), "+s"(out), "+s"(ws));
  const __amdgpu_buffer_rsrc_t rw = __builtin_amdgcn_make_buffer_rsrc((void*)ws, 0, WS_END, RSRC3);
  const __amdgpu_buffer_rsrc_t rx = __builtin_amdgcn_make_buffer_rsrc((void*)x, 0, 2 * D * 2, RSRC3);
  constexpr float SCALE = 0.07216878364870322f, LOG2E = 1.4426950408889634f;
  unsigned vzero;
  asm volatile("v_mov_b32 %0, 0" : "=v"(vzero));

  f32x2 fw1, fb1, fw2; float gw1, gb1, gw2, fb2;
  unsigned xres;
  unsigned short a1[3], a2[3], a3[3], a4;
#define LOAD_SMALL() { \
    const __amdgpu_buffer_rsrc_t r1 = __builtin_amdgcn_make_buffer_rsrc((void*)w1, 0, FF * 2 + 4, RSRC3); \
    const __amdgpu_buffer_rsrc_t r2 = __builtin_amdgcn_make_buffer_rsrc((void*)b1, 0, FF * 2 + 4, RSRC3); \
    const __amdgpu_buffer_rsrc_t r3 = __builtin_amdgcn_make_buffer_rsrc((void*)w2, 0, FF * 2 + 4, RSRC3); \
    _Pragma("unroll") for (int k = 0; k < 3; ++k) { \
      a1[k] = __builtin_amdgcn_raw_buffer_load_b16(r1, tid * 2 + 1024 * k, 0, 0); \
      a2[k] = __builtin_amdgcn_raw_buffer_load_b16(r2, tid * 2 + 1024 * k, 0, 0); \
      a3[k] = __builtin_amdgcn_raw_buffer_load_b16(r3, tid * 2 + 1024 * k, 0, 0); \
    } \
    a4 = __builtin_amdgcn_raw_buffer_load_b16(__builtin_amdgcn_make_buffer_rsrc((void*)b2, 0, 4, RSRC3), vzero, 0, 0); \
    xres = __builtin_amdgcn_raw_buffer_load_b32(rx, tid >= 192 ? (tid - 192) * 4 : OOB, b * D * 2, 0); \
    MEMBAR(); }
#define CONV_SMALL() { \
    fw1 = f32x2{bflo(a1[0]), bflo(a1[1])}; fb1 = f32x2{bflo(a2[0]), bflo(a2[1])}; fw2 = f32x2{bflo(a3[0]), bflo(a3[1])}; \
    gw1 = bflo(a1[2]); gb1 = bflo(a2[2]); gw2 = bflo(a3[2]); fb2 = bflo(a4); \
    asm volatile("" : "+v"(fw1), "+v"(fb1), "+v"(fw2), "+v"(gw1), "+v"(gb1), "+v"(gw2), "+v"(fb2), "+v"(xres) :: "memory"); }

#define LDS_WAIT(IDX, VAL, SLEEP) { unsigned spins_ = 0; \
    _Pragma("nounroll") while (__hip_atomic_load(ldu + L_CNT + (IDX), __ATOMIC_ACQUIRE, __HIP_MEMORY_SCOPE_WORKGROUP) != (unsigned)(VAL) && ++spins_ < (1u << 22)) \
      { if (SLEEP) __builtin_amdgcn_s_sleep(SLEEP); } }
#define LDS_ARRIVE(IDX) __hip_atomic_fetch_add(ldu + L_CNT + (IDX), 1u, __ATOMIC_ACQ_REL, __HIP_MEMORY_SCOPE_WORKGROUP)
#define PUBLISH(BB) { \
    const int d4_ = lane < 48 ? 4 * lane : 0; \
    const f32x4 ml01 = *reinterpret_cast<const f32x4*>(lds + L_ML + (BB) * 8), ml23 = *reinterpret_cast<const f32x4*>(lds + L_ML + (BB) * 8 + 4); \
    f32x4 v_[NSW]; \
    _Pragma("unroll") for (int w_ = 0; w_ < NSW; ++w_) v_[w_] = *reinterpret_cast<const f32x4*>(lds + L_RED + ((BB) * NSW + w_) * DH + d4_); \
    const float M_ = __uint_as_float(vbit(fmaxf(fmaxf(ml01.x, ml01.z), fmaxf(ml23.x, ml23.z)))); \
    const float f0 = __builtin_amdgcn_exp2f(ml01.x - M_), f1 = __builtin_amdgcn_exp2f(ml01.z - M_); \
    const float f2 = __builtin_amdgcn_exp2f(ml23.x - M_), f3 = __builtin_amdgcn_exp2f(ml23.z - M_); \
    f32x4 o_ = (v_[0] * f0 + v_[1] * f1) + (v_[2] * f2 + v_[3] * f3); \
    const float Ls_ = (ml01.y * f0 + ml01.w * f1) + (ml23.y * f2 + ml23.w * f3); \
    if (lane >= 48) { o_.x = M_; o_.y = Ls_; } \
    ST128((u32x4{vbit(o_.x), vbit(o_.y), vbit(o_.z), vbit(o_.w)}), rw, lane <= 48 ? lane * 16 : OOB, WS_P + bid * 2048 + (BB) * 1024); }
  u32x4 wop[6][2];
  unsigned* const wl = ldu + L_WO + ((wave & 3) * 12 * 64 + lane) * 4;
  if (wave >= NH) {
    const int sw = wave - NH;
    const size_t kvbase = (size_t)h * HEAD_BYTES;
    constexpr unsigned ROW1 = H * HEAD_BYTES;
    const __amdgpu_buffer_rsrc_t rk = __builtin_amdgcn_make_buffer_rsrc((void*)(kc + kvbase), 0, ROW1 + HEAD_BYTES, RSRC3);
    const __amdgpu_buffer_rsrc_t rv = __builtin_amdgcn_make_buffer_rsrc((void*)(vc + kvbase), 0, ROW1 + HEAD_BYTES, RSRC3);
    const unsigned kvo = r * (DH * 2) + c * 16;
    u32x4 buf[RPRE][3];
    unsigned so = (pq * NSW + sw) * TILE_BYTES + (h & 1) * RA * STRIDE;
    unsigned wb = 0;
    static_assert(RA + RPRE + 2 < WSTEPS && RPRE % 2 == 0 && RPRE >= 2, "no wrap before the main loop");
#pragma unroll
    for (int j = 0; j < RPRE; ++j) {
      if (j >= 2) { WAIT_VM3(); SB(); }
#pragma unroll
      for (int t = 0; t < 3; ++t) { buf[j][t] = LD128(rk, kvo + t * 128, so, AUX_NT); SB(); }
      so += STRIDE;
    }
    LOAD_SMALL();
    STAMP(8);
    __syncthreads();
    u32x4 q[3];
#pragma unroll
    for (int t = 0; t < 3; ++t) q[t] = *reinterpret_cast<const u32x4*>(ldu + L_Q + (8 * t + c) * 4);
    STAMP(1);
    float* sc = lds + L_SC + sw * 512 + r;

#define PIN12(A) asm volatile("" : "+v"(A[0]), "+v"(A[1]), "+v"(A[2]), "+v"(A[3]), "+v"(A[4]), "+v"(A[5]), "+v"(A[6]), "+v"(A[7]), \
    "+v"(A[8]), "+v"(A[9]), "+v"(A[10]), "+v"(A[11]) :: "memory")
#define ADVANCE() { so += STRIDE; if (so >= HEAD_BYTES) so -= HEAD_BYTES; }
#define KSTEP(B, REFILL, RS, SLOT) { \
    float s0_ = 0.f, s1_ = 0.f, s2_ = 0.f; \
    _Pragma("unroll") for (int j_ = 0; j_ < 4; ++j_) { s0_ = dot2(B[0][j_], q[0][j_], s0_); s1_ = dot2(B[1][j_], q[1][j_], s1_); s2_ = dot2(B[2][j_], q[2][j_], s2_); } \
    asm volatile("" : "+v"(s0_), "+v"(s1_), "+v"(s2_) :: "memory"); \
    SB(); \
    if (REFILL) { _Pragma("unroll") for (int u_ = 0; u_ < 3; ++u_) B[u_] = LD128(RS, kvo + u_ * 128, wb + so, AUX_NT); ADVANCE(); } \
    SB(); \
    float s_ = s0_ + s1_ + s2_; \
    s_ += dpp<XOR1>(s_); s_ += dpp<XOR2>(s_); s_ += dpp<HMIR>(s_); \
    s_ = rbf(rbf(s_) * SCALE); \
    if (c == 0) sc[(SLOT) * 8] = s_; \
    MEMBAR(); \
    SB(); }
#define VSTEP(B, REFILL, P) { \
    f32x2 vf_[12]; \
    _Pragma("unroll") for (int t_ = 0; t_ < 3; ++t_) \
      _Pragma("unroll") for (int j_ = 0; j_ < 4; ++j_) vf_[t_ * 4 + j_] = bf2(B[t_][j_]); \
    PIN12(vf_); \
    SB(); \
    if (REFILL) { _Pragma("unroll") for (int u_ = 0; u_ < 3; ++u_) B[u_] = LD128(rv, kvo + u_ * 128, wb + so, AUX_NT); ADVANCE(); } \
    SB(); \
    _Pragma("unroll") for (int k_ = 0; k_ < 12; ++k_) acc[k_] = fma2(splat(P), vf_[k_], acc[k_]); \
    PIN12(acc); \
    SB(); }

    KSTEP(buf[0], 1, rk, 0);
    KSTEP(buf[1], 1, rk, 1);
#pragma unroll
    for (int jb = 2; jb < RPRE; ++jb) KSTEP(buf[jb], 0, rk, jb);
    sc += RPRE * 8;
#pragma nounroll
    for (int i = RPRE; i < STEPS - 2; i += 2) {
      if (i == WSTEPS - 2) wb = ROW1;
      if (i == WSTEPS) {
#pragma unroll
        for (int t = 0; t < 3; ++t) q[t] = *reinterpret_cast<const u32x4*>(ldu + L_Q + 96 + (8 * t + c) * 4);
      }
      KSTEP(buf[0], 1, rk, 0);
      KSTEP(buf[1], 1, rk, 1);
      sc += 16;
    }
    wb = 0;
    KSTEP(buf[0], 1, rv, 0);
    KSTEP(buf[1], 1, rv, 1);
    STAMP(10);
    {
      f32x4* const sp4 = reinterpret_cast<f32x4*>(lds + L_SC + sw * 512) + lane;
      f32x4 sa = sp4[0], sb = sp4[64];
      float ma = fmaxf(fmaxf(sa.x, sa.y), fmaxf(sa.z, sa.w)), mb = fmaxf(fmaxf(sb.x, sb.y), fmaxf(sb.z, sb.w));
      ma = fmaxf(ma, dpp<XOR1>(ma)); ma = fmaxf(ma, dpp<XOR2>(ma)); ma = fmaxf(ma, dpp<HMIR>(ma)); ma = fmaxf(ma, dpp<ROR8>(ma));
      mb = fmaxf(mb, dpp<XOR1>(mb)); mb = fmaxf(mb, dpp<XOR2>(mb)); mb = fmaxf(mb, dpp<HMIR>(mb)); mb = fmaxf(mb, dpp<ROR8>(mb));
      ma = max3216(ma); mb = max3216(mb);
#pragma unroll
      for (int k = 0; k < 4; ++k) {
        sa[k] = __builtin_amdgcn_exp2f((sa[k] - ma) * LOG2E);
        sb[k] = __builtin_amdgcn_exp2f((sb[k] - mb) * LOG2E);
      }
      sp4[0] = sa; sp4[64] = sb;
      float l = fold32((sa.x + sa.y) + (sa.z + sa.w), (sb.x + sb.y) + (sb.z + sb.w));
      l = row_sum16(fold16(l, l));
      if ((lane & 31) == 0) *reinterpret_cast<f32x2*>(lds + L_ML + (lane >> 5) * 8 + 2 * sw) = f32x2{(lane ? mb : ma) * LOG2E, l};
      MEMBAR();
    }
    f32x2 acc[12];
#pragma unroll
    for (int k = 0; k < 12; ++k) acc[k] = f32x2{0.f, 0.f};
    sc = lds + L_SC + sw * 512 + r;
#define FLUSH(BB, ARR) { \
      float* const red_ = lds + L_RED + ((BB) * NSW + sw) * DH + fidx; \
      _Pragma("unroll") for (int t_ = 0; t_ < 3; ++t_) { \
        const float z0 = fold32(acc[t_ * 4 + 0].x, acc[t_ * 4 + 2].x), z1 = fold32(acc[t_ * 4 + 0].y, acc[t_ * 4 + 2].y); \
        const float z2 = fold32(acc[t_ * 4 + 1].x, acc[t_ * 4 + 3].x), z3 = fold32(acc[t_ * 4 + 1].y, acc[t_ * 4 + 3].y); \
        red_[64 * t_] = fold8(fold16(z0, z2), fold16(z1, z3), bit3); \
      } \
      if (lane == 0) ARR = __hip_atomic_fetch_add(ldu + L_CNT + (BB), 1u, __ATOMIC_ACQ_REL, __HIP_MEMORY_SCOPE_WORKGROUP); }
#pragma nounroll
    for (int i = 0; i < STEPS - 2; i += 2) {
      const float p0 = sc[0], p1 = sc[8];
      sc += 16;
      if (i == WSTEPS - 2) wb = ROW1;
      SB();
      VSTEP(buf[0], 1, p0);
      VSTEP(buf[1], 1, p1);
      if (i == WSTEPS - 2) {
        unsigned unused = 0;
        FLUSH(0, unused);
        (void)unused;
#pragma unroll
        for (int k = 0; k < 12; ++k) acc[k] = f32x2{0.f, 0.f};
      }
    }
    {
      const float p0 = sc[0], p1 = sc[8];
      VSTEP(buf[0], 0, p0);
      VSTEP(buf[1], 0, p1);
    }
    STAMP(2);
    unsigned arrived = 0;
    FLUSH(1, arrived);
    if (__builtin_amdgcn_readfirstlane(arrived) == NSW - 1) {
      PUBLISH(1);
      MEMBAR();
      ldu[L_CNT + 5] = 1u;
    }
    STAMP(3);
    LDS_WAIT(4, NH + 1, 0);
#pragma unroll
    for (int i = 0; i < 6; ++i) {
      wop[i][0] = *reinterpret_cast<const u32x4*>(wl + (2 * i) * 256);
      wop[i][1] = *reinterpret_cast<const u32x4*>(wl + (2 * i + 1) * 256);
    }
    LDS_WAIT(5, 1, 0);
  } else {
    if (wave < 3 && !NOQ) {
      const __amdgpu_buffer_rsrc_t rq = __builtin_amdgcn_make_buffer_rsrc((void*)wq, 0, D * H * DH * 2, RSRC3);
      unsigned xs[2][5], xp[2];
#pragma unroll
      for (int bb = 0; bb < 2; ++bb) {
#pragma unroll
        for (int i = 0; i < 5; ++i) xs[bb][i] = __builtin_amdgcn_raw_buffer_load_b32(rx, lane * 4 + i * 256 + bb * D * 2, 0, 0);
        xp[bb] = __builtin_amdgcn_raw_buffer_load_b32(rx, (c < 5 ? (8 * c + r) * 4 : 0) + bb * D * 2, pq * 160, 0);
      }
      const u32x2 ep = __builtin_amdgcn_raw_buffer_load_b64(rw, vzero, WS_EPOCH, AUX_SC1);
      SB();
      u32x4 wqr[5][2];
      {
        const unsigned vo = (unsigned)(2 * r) * (H * DH * 2) + c * 16;
        const unsigned qso = (unsigned)(80 * pq) * (H * DH * 2) + h * (DH * 2) + wave * 128;
#pragma unroll
        for (int i = 0; i < 5; ++i)
#pragma unroll
          for (int e = 0; e < 2; ++e) { wqr[i][e] = LD128(rq, vo, qso + (16 * i + e) * (H * DH * 2), AUX_NT); SB(); }
      }
      float s0 = 0.f, s1 = 0.f, q0 = 0.f, q1 = 0.f;
#pragma unroll
      for (int i = 0; i < 5; ++i) {
        s0 = dot2(xs[0][i], ONES, s0); q0 = dot2(xs[0][i], xs[0][i], q0);
        s1 = dot2(xs[1][i], ONES, s1); q1 = dot2(xs[1][i], xs[1][i], q1);
      }
      const float st = row_sum16(fold16(fold32(s0, s1), fold32(q0, q1)));
      auto sw2 = __builtin_amdgcn_permlane16_swap(__float_as_uint(st), __float_as_uint(st), false, false);
      const float mean = __uint_as_float(sw2[0]) * (1.0f / D);
      const float var = fmaxf(fmaf(-mean, mean, __uint_as_float(sw2[1]) * (1.0f / D)), 0.f);
      const float inv = ln_den_inv(var), mb = rbf(mean);
      const float m0 = rdlane(mb, 0), m1 = rdlane(mb, 32), i0 = rdlane(inv, 0), i1 = rdlane(inv, 32);
      const unsigned xn0 = pack_bf16(rbf(bflo(xp[0]) - m0) * i0, rbf(bfhi(xp[0]) - m0) * i0);
      const unsigned xn1 = pack_bf16(rbf(bflo(xp[1]) - m1) * i1, rbf(bfhi(xp[1]) - m1) * i1);
      const unsigned long long tg = mix64(((unsigned long long)(unsigned)__builtin_amdgcn_readfirstlane(ep.y) << 32) | (unsigned)__builtin_amdgcn_readfirstlane(ep.x));
      const unsigned thi = (unsigned)(tg >> 32), t8 = (unsigned)tg & 0xff000000u;
      float a0[8], a1q[8];
#pragma unroll
      for (int k = 0; k < 8; ++k) { a0[k] = 0.f; a1q[k] = 0.f; }
#define QGROUP(I) { \
        unsigned b0_ = __builtin_amdgcn_update_dpp(0u, xn0, 0x150 + I, 0xf, 0x3, false); b0_ = __builtin_amdgcn_update_dpp(b0_, xn0, 0x158 + I, 0xf, 0xc, false); \
        unsigned b1_ = __builtin_amdgcn_update_dpp(0u, xn1, 0x150 + I, 0xf, 0x3, false); b1_ = __builtin_amdgcn_update_dpp(b1_, xn1, 0x158 + I, 0xf, 0xc, false); \
        _Pragma("unroll") for (int j = 0; j < 4; ++j) { \
          const unsigned plo = __builtin_amdgcn_perm(wqr[I][1][j], wqr[I][0][j], 0x05040100u), phi = __builtin_amdgcn_perm(wqr[I][1][j], wqr[I][0][j], 0x07060302u); \
          a0[2 * j] = dot2(plo, b0_, a0[2 * j]); a0[2 * j + 1] = dot2(phi, b0_, a0[2 * j + 1]); \
          a1q[2 * j] = dot2(plo, b1_, a1q[2 * j]); a1q[2 * j + 1] = dot2(phi, b1_, a1q[2 * j + 1]); } }
      QGROUP(0) QGROUP(1) QGROUP(2) QGROUP(3) QGROUP(4)
      float z[8];
#pragma unroll
      for (int k = 0; k < 8; ++k) z[k] = fold32(a0[k], a1q[k]);
      float y[4];
#pragma unroll
      for (int k = 0; k < 4; ++k) y[k] = fold16(z[k], z[k + 4]);
      const float u0 = fold8(y[0], y[2], bit3), u1 = fold8(y[1], y[3], bit3);
      const unsigned pidx = 32 * wave + 4 * c + 2 * ((lane >> 4) & 1) + (bit3 ? 1 : 0);
      ST128(pack2(u0, u1, t8, thi), rw, ((lane >> 5) * 8 * 96 + pidx) * 16, WS_QP + (h * 16 + pq) * (96 * 16));
      STAMP(8);
      asm volatile("s_waitcnt vmcnt(0)" ::: "memory");
      {
        const unsigned qo = ((lane >> 5) * 8 * 96 + 32 * wave + (lane & 31)) * 16;
        const unsigned qb = WS_QP + h * 16 * (96 * 16);
        u32x4 gq[8];
        for (unsigned spins = 0;; ++spins) {
          MEMBAR();
#pragma unroll
          for (int k = 0; k < 8; ++k) gq[k] = LD128(rw, qo, qb + k * (96 * 16), AUX_SC1);
          unsigned bad = 0;
#pragma unroll
          for (int k = 0; k < 8; ++k) bad |= tag_bad(gq[k], t8, thi);
          if (__all(bad == 0u) || spins > SPIN_CAP) break;
        }
        const f32x2 qq = ((g01(gq[0]) + g01(gq[2])) + (g01(gq[4]) + g01(gq[6]))) + ((g01(gq[1]) + g01(gq[3])) + (g01(gq[5]) + g01(gq[7])));
        ldu[L_Q + (lane >> 5) * 96 + 32 * wave + (lane & 31)] = pack_bf16(qq.x, qq.y);
      }
      if (tid == 0) *reinterpret_cast<u32x2*>(ldu + L_TAG) = u32x2{(unsigned)tg, thi};
    }
    const unsigned yoff = ((lane >> 5) * 8 * 320 + (lane & 31)) * 16;
    const unsigned ybo = WS_Y + (team * 320 + pc * 32) * 16 + b * 8;
    if (wave == 3) {
      if (lane < 8) ldu[L_CNT + lane] = 0u;
      const u32x2 ep = LD64(rw, vzero, WS_EPOCH, AUX_SC1);
      ST128((u32x4{0u, 0u, 0u, 0u}), rw, lane * 16, WS_P + bid * 2048);
      ST128((u32x4{0u, 0u, 0u, 0u}), rw, lane * 16, WS_P + bid * 2048 + 1024);
      ST64((u32x2{0u, 0u}), rw, proj ? yoff : OOB, ybo);
      asm volatile("s_waitcnt vmcnt(0)" ::: "memory");
      const unsigned long long tg = mix64(((unsigned long long)(unsigned)__builtin_amdgcn_readfirstlane(ep.y) << 32) | (unsigned)__builtin_amdgcn_readfirstlane(ep.x));
      ST64((u32x2{(unsigned)tg, (unsigned)(tg >> 32)}), rw, lane == 0 ? 0u : OOB, WS_RDY + (team * 32 + jj) * 8);
    }
    if (NOQ && wave == 0) {
      const u32x2 ep = __builtin_amdgcn_raw_buffer_load_b64(rw, vzero, WS_EPOCH, AUX_SC1);
      const unsigned long long tg = mix64(((unsigned long long)(unsigned)__builtin_amdgcn_readfirstlane(ep.y) << 32) | (unsigned)__builtin_amdgcn_readfirstlane(ep.x));
      if (tid == 0) *reinterpret_cast<u32x2*>(ldu + L_TAG) = u32x2{(unsigned)tg, (unsigned)(tg >> 32)};
      ldu[L_Q + lane] = 0x3c003c00; ldu[L_Q + 64 + lane] = 0x3c003c00; ldu[L_Q + 128 + lane] = 0x3c003c00;
    }
    STAMP(9);
    __syncthreads();
    STAMP(1);
    LOAD_SMALL();
    for (int i = 0; i < WOST; ++i) __builtin_amdgcn_s_sleep(127);
    const __amdgpu_buffer_rsrc_t ro = __builtin_amdgcn_make_buffer_rsrc((void*)wo, 0, D * H * DH * 2, RSRC3);
    const unsigned vo = (unsigned)(2 * r) * (D * 2) + c * 16;
    const unsigned wso = (NOWO || !proj) ? OOB : (unsigned)(768 * team + 384 * b + 96 * wave) * (D * 2) + pc * 128;
#pragma unroll
    for (int i = 0; i < 6; ++i) {
      if (WOSL) __builtin_amdgcn_s_sleep(WOSL);
      const u32x4 e0 = LD128(ro, vo, wso + 16 * i * (D * 2), AUX_NT);
      const u32x4 e1 = LD128(ro, vo + D * 2, wso + 16 * i * (D * 2), AUX_NT);
      u32x4 p0, p1;
      p0.x = __builtin_amdgcn_perm(e1.x, e0.x, 0x05040100u); p0.y = __builtin_amdgcn_perm(e1.x, e0.x, 0x07060302u);
      p0.z = __builtin_amdgcn_perm(e1.y, e0.y, 0x05040100u); p0.w = __builtin_amdgcn_perm(e1.y, e0.y, 0x07060302u);
      p1.x = __builtin_amdgcn_perm(e1.z, e0.z, 0x05040100u); p1.y = __builtin_amdgcn_perm(e1.z, e0.z, 0x07060302u);
      p1.z = __builtin_amdgcn_perm(e1.w, e0.w, 0x05040100u); p1.w = __builtin_amdgcn_perm(e1.w, e0.w, 0x07060302u);
      wop[i][0] = p0; wop[i][1] = p1;
      *reinterpret_cast<u32x4*>(wl + (2 * i) * 256) = p0;
      *reinterpret_cast<u32x4*>(wl + (2 * i + 1) * 256) = p1;
      MEMBAR();
      SB();
    }
    if (lane == 0) LDS_ARRIVE(4);
    if (wave == 0) {
      const u32x2 tg = *reinterpret_cast<const u32x2*>(ldu + L_TAG);
      for (unsigned spins = 0; spins < SPIN_CAP; ++spins) {
        MEMBAR();
        const u32x4 r0 = LD128(rw, lane * 16, WS_RDY, AUX_SC1), r1 = LD128(rw, lane * 16 + 1024, WS_RDY, AUX_SC1);
        const unsigned bad = (r0.x ^ tg.x) | (r0.y ^ tg.y) | (r0.z ^ tg.x) | (r0.w ^ tg.y) | (r1.x ^ tg.x) | (r1.y ^ tg.y) | (r1.z ^ tg.x) | (r1.w ^ tg.y);
        if (__all(bad == 0u)) break;
        __builtin_amdgcn_s_sleep(32);
      }
      if (lane == 0) LDS_ARRIVE(4);
    }
    LDS_WAIT(0, NSW, 16);
    if (wave == 1) PUBLISH(0);
    for (int i = 0; i < EDELAY; ++i) __builtin_amdgcn_s_sleep(127);
    LDS_WAIT(4, NH + 1, 16);
  }
  const u32x2 tg2 = *reinterpret_cast<const u32x2*>(ldu + L_TAG);
  const unsigned tlo = __builtin_amdgcn_readfirstlane(tg2.x), thi = __builtin_amdgcn_readfirstlane(tg2.y);

  if (proj) {
    const unsigned pbase = WS_P + (unsigned)(team + 64 * (2 * b + ((wave >> 1) & 1))) * 2048 + (wave >> 2) * 1024;
    const unsigned po = ((wave & 1) * 48 + (lane < 48 ? lane : 0)) * 8;
    const unsigned mo = (lane & 7) * (8 * 2048) + 96 * 8;
    u32x2 go[GS], gm;
    MEMBAR();
    gm = LD64(rw, mo, pbase, AUX_SC1);
#pragma unroll
    for (int s = 0; s < GS; ++s) go[s] = LD64(rw, po, pbase + s * (8 * 2048), AUX_SC1);
    MEMBAR();
    unsigned pend = 0, spins = 0;
    if (!__all((gm.x & gm.y & 1u) != 0u)) pend |= 1u << GS;
#pragma unroll
    for (int s = 0; s < GS; ++s)
      if (!__all((go[s].x & go[s].y & 1u) != 0u)) pend |= 1u << s;
    while (pend != 0u && ++spins < SPIN_CAP) {
      if (wave < NH) __builtin_amdgcn_s_sleep(64);
      MEMBAR();
      if (pend & (1u << GS)) gm = LD64(rw, mo, pbase, AUX_SC1);
#pragma unroll
      for (int s = 0; s < GS; ++s)
        if (pend & (1u << s)) go[s] = LD64(rw, po, pbase + s * (8 * 2048), AUX_SC1);
      if (pend & (1u << GS)) { if (__all((gm.x & gm.y & 1u) != 0u)) pend &= ~(1u << GS); }
#pragma unroll
      for (int s = 0; s < GS; ++s)
        if (pend & (1u << s)) { if (__all((go[s].x & go[s].y & 1u) != 0u)) pend &= ~(1u << s); }
    }
    const float ms = __uint_as_float(gm.x);
    float M = fmaxf(ms, dpp<XOR1>(ms)); M = fmaxf(M, dpp<XOR2>(M)); M = fmaxf(M, dpp<HMIR>(M));
    const float f = __builtin_amdgcn_exp2f(ms - M);
    float Ls = f * __uint_as_float(gm.y);
    Ls += dpp<XOR1>(Ls); Ls += dpp<XOR2>(Ls); Ls += dpp<HMIR>(Ls);
    const float wgt = f * __builtin_amdgcn_rcpf(Ls);
    f32x2 oa = splat(rdlane(wgt, 0)) * asf2(go[0].x, go[0].y), ob = splat(rdlane(wgt, 1)) * asf2(go[1].x, go[1].y);
#pragma unroll
    for (int s = 2; s < GS; s += 2) {
      oa = fma2(splat(rdlane(wgt, s)), asf2(go[s].x, go[s].y), oa); ob = fma2(splat(rdlane(wgt, s + 1)), asf2(go[s + 1].x, go[s + 1].y), ob);
    }
    oa += ob;
    if (lane < 48) ldu[L_O + 48 * wave + lane] = pack_bf16(oa.x, oa.y);
  }
  STAMP(4);
  if (proj) {
    unsigned op[6];
#pragma unroll
    for (int i = 0; i < 6; ++i) op[i] = ldu[L_O + 48 * wave + 8 * i + r];
    float y[8];
#pragma unroll
    for (int k = 0; k < 8; ++k) y[k] = 0.f;
#pragma unroll
    for (int i = 0; i < 6; ++i) {
#pragma unroll
      for (int k = 0; k < 4; ++k) { y[k] = dot2(wop[i][0][k], op[i], y[k]); y[k + 4] = dot2(wop[i][1][k], op[i], y[k + 4]); }
    }
    const float z0 = fold32(y[0], y[4]), z1 = fold32(y[1], y[5]), z2 = fold32(y[2], y[6]), z3 = fold32(y[3], y[7]);
    const float u = fold8(fold16(z0, z2), fold16(z1, z3), bit3);
    lds[L_YR + wave * 64 + fidx] = u;
    const int bb = wave >> 2;
    unsigned arrived = 0;
    if (lane == 0) arrived = LDS_ARRIVE(2 + bb);
    if (__builtin_amdgcn_readfirstlane(arrived) == 3) {
      const int t = lane & 31;
      f32x2 v[4];
#pragma unroll
      for (int w = 0; w < 4; ++w) v[w] = *reinterpret_cast<const f32x2*>(lds + L_YR + (4 * bb + w) * 64 + 2 * t);
      const f32x2 ys = (v[0] + v[1]) + (v[2] + v[3]);
      ST64((u32x2{vbit(ys.x), vbit(ys.y)}), rw, lane < 32 ? lane * 16 : OOB, WS_Y + ((bb * 8 + team) * 320 + pc * 32) * 16 + b * 8);
    }
  }
  STAMP(5);
  __syncthreads();
  CONV_SMALL();
  if (wave >= 3) {
    u32x4 gy[8];
    const unsigned yo = (tid - 192) * 16, ybase = WS_Y + b * (8 * 320 * 16);
    unsigned pend = 0xffu, spins = 0;
    do {
      MEMBAR();
#pragma unroll
      for (int s = 0; s < 8; ++s)
        if (pend & (1u << s)) gy[s] = LD128(rw, yo, ybase + s * (320 * 16), AUX_SC1);
#pragma unroll
      for (int s = 0; s < 8; ++s)
        if (pend & (1u << s)) { if (__all((gy[s].x & gy[s].y & gy[s].z & gy[s].w & 1u) != 0u)) pend &= ~(1u << s); }
    } while (pend != 0u && ++spins < SPIN_CAP);
    f32x2 ap[8];
#pragma unroll
    for (int s = 0; s < 8; ++s) ap[s] = asf2(gy[s].x, gy[s].y) + asf2(gy[s].z, gy[s].w);
    const f32x2 a = ((ap[0] + ap[1]) + (ap[2] + ap[3])) + ((ap[4] + ap[5]) + (ap[6] + ap[7]));
    const unsigned hp = pack_bf16(bflo(xres) + rbf(a.x), bfhi(xres) + rbf(a.y));
    ldu[L_H + tid - 192] = hp;
    float st = fold32(dot2(hp, ONES, 0.f), dot2(hp, hp, 0.f));
    st = row_sum16(fold16(st, st));
    if ((lane & 31) == 0) lds[L_ST + (wave - 3) * 2 + (lane >> 5)] = st;
  }
  __syncthreads();
  STAMP(6);
  const int d0 = 5 * (team * 16 + cb);
  float he;
  {
    const int de = d0 + (lane < 5 ? lane : 0);
    const unsigned hw = ldu[L_H + (de >> 1)];
    f32x2 sv[5];
#pragma unroll
    for (int w = 0; w < 5; ++w) sv[w] = *reinterpret_cast<const f32x2*>(lds + L_ST + 2 * w);
    const f32x2 st = (((sv[0] + sv[1]) + (sv[2] + sv[3])) + sv[4]) * splat(1.0f / D);
    he = (de & 1) ? bfhi(hw) : bflo(hw);
    const float mean1 = st.x;
    const float var1 = fmaxf(fmaf(-mean1, mean1, st.y), 0.f);
    const float i1 = ln_den_inv(var1), m1 = rbf(mean1);
    const float m2 = rbf((mean1 - m1) * i1), i2 = ln_den_inv(var1 * i1 * i1);
    const float tl = rbf(rbf(rbf(rbf(he - m1) * i1) - m2) * i2);
    constexpr float G1 = -2.0f * 0.7978845608028654f * LOG2E, G3 = G1 * 0.044715f;
    float te[5];
#pragma unroll
    for (int e = 0; e < 5; ++e) te[e] = rdlane(tl, e);
    f32x2 u[8], z[8];
#pragma unroll
    for (int e = 0; e < 5; ++e) u[e] = fma2(splat(te[e]), fw1, fb1);
    u[5] = fma2(f32x2{te[0], te[1]}, splat(gw1), splat(gb1));
    u[6] = fma2(f32x2{te[2], te[3]}, splat(gw1), splat(gb1));
    u[7] = fma2(splat(te[4]), splat(gw1), splat(gb1));
    SB();
#pragma unroll
    for (int k = 0; k < 8; ++k) z[k] = u[k] * u[k];
    SB();
#pragma unroll
    for (int k = 0; k < 8; ++k) z[k] = fma2(z[k], splat(G3), splat(G1));
    SB();
#pragma unroll
    for (int k = 0; k < 8; ++k) z[k] = z[k] * u[k];
    SB();
#pragma unroll
    for (int k = 0; k < 8; ++k) { z[k].x = __builtin_amdgcn_exp2f(z[k].x); if (k < 7) z[k].y = __builtin_amdgcn_exp2f(z[k].y); }
#pragma unroll
    for (int k = 0; k < 8; ++k) u[k] = u[k] * (k < 5 ? fw2 : splat(gw2));
    SB();
#pragma unroll
    for (int k = 0; k < 8; ++k) z[k] = z[k] + splat(1.0f);
    SB();
#pragma unroll
    for (int k = 0; k < 8; ++k) { z[k].x = __builtin_amdgcn_rcpf(z[k].x); if (k < 7) z[k].y = __builtin_amdgcn_rcpf(z[k].y); }
    SB();
#pragma unroll
    for (int k = 0; k < 8; ++k) u[k] = u[k] * z[k];
    const float ya0 = (u[0].x + u[0].y) + u[5].x, ya1 = (u[1].x + u[1].y) + u[5].y, ya2 = (u[2].x + u[2].y) + u[6].x,
                ya3 = (u[3].x + u[3].y) + u[6].y, ya4 = (u[4].x + u[4].y) + u[7].x;
    const float f4 = fold32(ya4, ya4);
    float r0 = fold8(fold16(fold32(ya0, ya1), fold32(ya2, ya3)), fold16(f4, f4), bit3);
    r0 += dpp<XOR1>(r0); r0 += dpp<XOR2>(r0); r0 += dpp<HMIR>(r0);
    const int el = bit3 ? 4 : (lane >> 5) + 2 * ((lane >> 4) & 1);
    if ((lane & 15) == 0 || lane == 8) lds[L_MR + el * 8 + wave] = r0;
  }
  __syncthreads();
  if (tid < 5) {
    const f32x4 pa = *reinterpret_cast<const f32x4*>(lds + L_MR + tid * 8), pb = *reinterpret_cast<const f32x4*>(lds + L_MR + tid * 8 + 4);
    const f32x4 ps = pa + pb;
    const float yv = (ps.x + ps.y) + (ps.z + ps.w);
    const unsigned o = pack_bf16(he + rbf(rbf(yv) + fb2), 0.f);
    __builtin_amdgcn_raw_buffer_store_b16((unsigned short)o, __builtin_amdgcn_make_buffer_rsrc((void*)out, 0, 2 * D * 2 + 4, RSRC3),
                                          tid * 2, (b * D + d0) * 2, 0);
  }
  if (tid == 0 && bid == 0)
    __builtin_amdgcn_raw_buffer_store_b64(u32x2{tlo, thi}, rw, 0, WS_EPOCH, AUX_SC1);
  STAMP(7);
}

}

extern "C" void kernel_launch(void* const* d_in, const int* in_sizes, int n_in, void* d_out, int out_size, void* d_ws,
                              size_t ws_size, hipStream_t stream) {
  (void)in_sizes; (void)n_in; (void)out_size; (void)ws_size;
  fused_kernel<<<dim3(NWG), dim3(NTHR), 0, stream>>>(
      (const char*)d_in[0], (const char*)d_in[1], (const char*)d_in[2], (const char*)d_in[3], (const char*)d_in[4],
      (const char*)d_in[6], (const char*)d_in[7], (const char*)d_in[8], (const char*)d_in[9], (char*)d_out, (char*)d_ws);
}
```

```cpp
#include <hip/hip_runtime.h>
#include <hip/hip_bf16.h>
#include <stdint.h>

namespace {

typedef unsigned u32x4 __attribute__((ext_vector_type(4)));
typedef unsigned u32x2 __attribute__((ext_vector_type(2)));
typedef float f32x2 __attribute__((ext_vector_type(2)));
typedef float f32x4 __attribute__((ext_vector_type(4)));
typedef __bf16 bf16x2 __attribute__((ext_vector_type(2)));

constexpr int D = 640, H = 32, SK = 8192, DH = 192, FF = 1536;
constexpr int NWG = 256, NTHR = 512, NWAVE = 8;
constexpr unsigned TILE_BYTES = 8 * DH * 2;
constexpr unsigned HEAD_BYTES = SK * DH * 2;
constexpr int RA = 8;
constexpr int RPRE = 8;
constexpr int WOSLOT = 9;
constexpr int GS = 8;
constexpr int NSW = 4, NH = NWAVE - NSW;
constexpr int WSTEPS = SK / 8 / (GS * NSW);
constexpr int STEPS = 2 * WSTEPS;
constexpr unsigned STRIDE = GS * NSW * TILE_BYTES;
constexpr int AUX_NT = 2, AUX_SC1 = 16;
constexpr unsigned RSRC3 = 0x00020000u;
constexpr unsigned OOB = 0x80000000u;

constexpr unsigned WS_EPOCH = 0;
constexpr unsigned WS_RDY = 4096;
constexpr unsigned WS_P = 8192;
constexpr unsigned WS_Y = WS_P + NWG * 2048;
constexpr unsigned WS_QP = WS_Y + 2 * 8 * 320 * 16;
constexpr unsigned WS_END = WS_QP + 32 * 2 * 8 * 96 * 16;

constexpr int L_RED = 0;
constexpr int L_ML = L_RED + 2 * NSW * DH;
constexpr int L_Q = L_ML + 16;
constexpr int L_TAG = L_Q + 192;
constexpr int L_CNT = L_TAG + 4;
constexpr int L_W = L_CNT + 8;
constexpr int L_F = L_W + 32;
constexpr int L_O = L_F + 4;
constexpr int L_YR = L_O + 384;
constexpr int L_H = L_YR + 512;
constexpr int L_ST = L_H + 320;
constexpr int L_MR = L_ST + 16;
constexpr int L_SC = L_MR + 64;
constexpr int L_WO = L_SC + NSW * 512;
constexpr int L_TOTAL = 24 * 1024;
static_assert(L_WO + 24 * 2 * 64 * 4 <= L_TOTAL, "lds");
static_assert(L_WO % 4 == 0 && L_Q % 4 == 0 && L_ST % 4 == 0 && L_MR % 4 == 0, "alignment");

__device__ __forceinline__ float dot2(unsigned a, unsigned b, float c) {
  return __builtin_amdgcn_fdot2_f32_bf16(__builtin_bit_cast(bf16x2, a), __builtin_bit_cast(bf16x2, b), c, false);
}
template <int CTRL> __device__ __forceinline__ float dpp(float x) {
  return __builtin_bit_cast(float, __builtin_amdgcn_mov_dpp(__builtin_bit_cast(int, x), CTRL, 0xf, 0xf, true));
}
constexpr int XOR1 = 0xB1, XOR2 = 0x4E, HMIR = 0x141, ROR8 = 0x128;
constexpr unsigned ONES = 0x3f803f80u;
__device__ __forceinline__ float bflo(unsigned u) { return __builtin_bit_cast(float, u << 16); }
__device__ __forceinline__ float bfhi(unsigned u) { return __builtin_bit_cast(float, u & 0xffff0000u); }
__device__ __forceinline__ f32x2 bf2(unsigned u) { return f32x2{bflo(u), bfhi(u)}; }
__device__ __forceinline__ unsigned pack_bf16(float lo, float hi) {
  bf16x2 v; v[0] = (__bf16)lo; v[1] = (__bf16)hi;
  return __builtin_bit_cast(unsigned, v);
}
__device__ __forceinline__ float rbf(float x) {
  typedef float f32x2_ __attribute__((ext_vector_type(2)));
  return __builtin_bit_cast(float, __builtin_convertvector(f32x2_{0.f, x}, bf16x2));
}
__device__ __forceinline__ float fold32(float a, float b) {
  auto r = __builtin_amdgcn_permlane32_swap(__float_as_uint(a), __float_as_uint(b), false, false);
  return __uint_as_float(r[0]) + __uint_as_float(r[1]);
}
__device__ __forceinline__ float fold16(float a, float b) {
  auto r = __builtin_amdgcn_permlane16_swap(__float_as_uint(a), __float_as_uint(b), false, false);
  return __uint_as_float(r[0]) + __uint_as_float(r[1]);
}
__device__ __forceinline__ float fold8(float a, float b, bool bit3) {
  const float keep = bit3 ? b : a, send = bit3 ? a : b;
  return keep + dpp<ROR8>(send);
}
__device__ __forceinline__ float max3216(float x) {
  auto s = __builtin_amdgcn_permlane16_swap(__float_as_uint(x), __float_as_uint(x), false, false);
  x = fmaxf(__uint_as_float(s[0]), __uint_as_float(s[1]));
  auto t = __builtin_amdgcn_permlane32_swap(__float_as_uint(x), __float_as_uint(x), false, false);
  return fmaxf(__uint_as_float(t[0]), __uint_as_float(t[1]));
}
__device__ __forceinline__ float row_sum16(float x) {
  x += dpp<XOR1>(x); x += dpp<XOR2>(x); x += dpp<HMIR>(x); x += dpp<ROR8>(x);
  return x;
}
__device__ __forceinline__ unsigned long long mix64(unsigned long long z) {
  z += 0x9E3779B97F4A7C15ull;
  z = (z ^ (z >> 30)) * 0xBF58476D1CE4E5B9ull;
  z = (z ^ (z >> 27)) * 0x94D049BB133111EBull;
  return z ^ (z >> 31);
}
__device__ __forceinline__ u32x4 pack2(float a, float b, unsigned t8, unsigned thi) {
  u32x4 r; r.x = (__float_as_uint(a) >> 8) | t8; r.y = thi; r.z = (__float_as_uint(b) >> 8) | t8; r.w = thi; return r;
}
__device__ __forceinline__ unsigned tag_bad(u32x4 g, unsigned t8, unsigned thi) {
  return (((g.x ^ t8) | (g.z ^ t8)) >> 24) | (g.y ^ thi) | (g.w ^ thi);
}
__device__ __forceinline__ float g0(u32x4 g) { return __uint_as_float(g.x << 8); }
__device__ __forceinline__ float g1(u32x4 g) { return __uint_as_float(g.z << 8); }
__device__ __forceinline__ f32x2 g01(u32x4 g) { return f32x2{g0(g), g1(g)}; }
__device__ __forceinline__ unsigned vbit(float x) { return __float_as_uint(x) | 1u; }
__device__ __forceinline__ f32x2 asf2(unsigned a, unsigned b) { return f32x2{__uint_as_float(a), __uint_as_float(b)}; }
__device__ __forceinline__ float ln_den_inv(float var) {
  return __builtin_amdgcn_rcpf(rbf(__builtin_amdgcn_sqrtf(rbf(rbf(var) + 1e-5f))));
}
__device__ __forceinline__ float rdlane(float x, int l) { return __uint_as_float(__builtin_amdgcn_readlane(__float_as_uint(x), l)); }
__device__ __forceinline__ f32x2 splat(float x) { return f32x2{x, x}; }
__device__ __forceinline__ f32x2 fma2(f32x2 a, f32x2 b, f32x2 c) { return __builtin_elementwise_fma(a, b, c); }

#define SB() __builtin_amdgcn_sched_barrier(0)
#define MEMBAR() asm volatile("" ::: "memory")
#define LD128(rs, vo, so, aux) __builtin_amdgcn_raw_buffer_load_b128((rs), (vo), (so), (aux))
#define ST128(v, rs, vo, so) __builtin_amdgcn_raw_buffer_store_b128((v), (rs), (vo) + (so), 0, AUX_SC1)
#define LD64(rs, vo, so, aux) __builtin_amdgcn_raw_buffer_load_b64((rs), (vo), (so), (aux))
#define ST64(v, rs, vo, so) __builtin_amdgcn_raw_buffer_store_b64((v), (rs), (vo), (so), AUX_SC1)
#define WAIT_VM3() __builtin_amdgcn_s_waitcnt(0x0F73)

constexpr unsigned SPIN_CAP = 1u << 17;

__global__ void __launch_bounds__(NTHR, 2)
fused_kernel(const char* x, const char* kc, const char* vc, const char* wq, const char* wo, const char* w1,
             const char* b1, const char* w2, const char* b2, char* out, char* ws) {
  __shared__ __attribute__((aligned(16))) float lds[L_TOTAL];
  unsigned* const ldu = reinterpret_cast<unsigned*>(lds);
  const int tid = threadIdx.x, lane = tid & 63, wave = __builtin_amdgcn_readfirstlane(tid >> 6);
  const int bid = blockIdx.x, team = bid & 7, jj = bid >> 3, hl = jj >> 3, b = (jj >> 2) & 1, sp = jj & 3;
  const int h = team * 4 + hl, cb = hl * 4 + sp, pq = jj & 7;
  const int pc = sp * 4 + hl;
  const bool proj = pc < 10;
  const int qh = jj, qp = team;
  const int r = lane >> 3, c = lane & 7;
  const bool bit3 = (lane & 8) != 0;
  const int fidx = 8 * c + 4 * (lane >> 5) + 2 * ((lane >> 4) & 1) + (bit3 ? 1 : 0);
  asm volatile("" : "+s"(x), "+s"(kc), "+s"(vc), "+s"(wq), "+s"(wo), "+s"(w1), "+s"(b1), "+s"(w2), "+s"(b2), "+s"(out), "+s"(ws));
  const __amdgpu_buffer_rsrc_t rw = __builtin_amdgcn_make_buffer_rsrc((void*)ws, 0, WS_END, RSRC3);
  const __amdgpu_buffer_rsrc_t rx = __builtin_amdgcn_make_buffer_rsrc((void*)x, 0, 2 * D * 2, RSRC3);
  constexpr float SCALE = 0.07216878364870322f, LOG2E = 1.4426950408889634f;
  unsigned vzero;
  asm volatile("v_mov_b32 %0, 0" : "=v"(vzero));

  f32x2 fw1, fb1, fw2; float gw1, gb1, gw2, fb2;
  unsigned xres;
  unsigned short a1[3], a2[3], a3[3], a4;
#define LOAD_SMALL() { \
    const __amdgpu_buffer_rsrc_t r1 = __builtin_amdgcn_make_buffer_rsrc((void*)w1, 0, FF * 2 + 4, RSRC3); \
    const __amdgpu_buffer_rsrc_t r2 = __builtin_amdgcn_make_buffer_rsrc((void*)b1, 0, FF * 2 + 4, RSRC3); \
    const __amdgpu_buffer_rsrc_t r3 = __builtin_amdgcn_make_buffer_rsrc((void*)w2, 0, FF * 2 + 4, RSRC3); \
    _Pragma("unroll") for (int k = 0; k < 3; ++k) { \
      a1[k] = __builtin_amdgcn_raw_buffer_load_b16(r1, tid * 2 + 1024 * k, 0, 0); \
      a2[k] = __builtin_amdgcn_raw_buffer_load_b16(r2, tid * 2 + 1024 * k, 0, 0); \
      a3[k] = __builtin_amdgcn_raw_buffer_load_b16(r3, tid * 2 + 1024 * k, 0, 0); \
    } \
    a4 = __builtin_amdgcn_raw_buffer_load_b16(__builtin_amdgcn_make_buffer_rsrc((void*)b2, 0, 4, RSRC3), vzero, 0, 0); \
    xres = __builtin_amdgcn_raw_buffer_load_b32(rx, tid >= 192 ? (tid - 192) * 4 : OOB, b * D * 2, 0); \
    MEMBAR(); }
#define CONV_SMALL() { \
    fw1 = f32x2{bflo(a1[0]), bflo(a1[1])}; fb1 = f32x2{bflo(a2[0]), bflo(a2[1])}; fw2 = f32x2{bflo(a3[0]), bflo(a3[1])}; \
    gw1 = bflo(a1[2]); gb1 = bflo(a2[2]); gw2 = bflo(a3[2]); fb2 = bflo(a4); \
    asm volatile("" : "+v"(fw1), "+v"(fb1), "+v"(fw2), "+v"(gw1), "+v"(gb1), "+v"(gw2), "+v"(fb2), "+v"(xres) :: "memory"); }

#define LDS_WAIT(IDX, VAL, SLEEP) { unsigned spins_ = 0; \
    _Pragma("nounroll") while (__hip_atomic_load(ldu + L_CNT + (IDX), __ATOMIC_ACQUIRE, __HIP_MEMORY_SCOPE_WORKGROUP) != (unsigned)(VAL) && ++spins_ < (1u << 22)) \
      { if (SLEEP) __builtin_amdgcn_s_sleep(SLEEP); } }
#define LDS_ARRIVE(IDX) __hip_atomic_fetch_add(ldu + L_CNT + (IDX), 1u, __ATOMIC_ACQ_REL, __HIP_MEMORY_SCOPE_WORKGROUP)
  constexpr int SETUP_DONE = 2 * NH + 2;
  u32x4 wop[6][2];
  f32x4 wv;
  unsigned* const wl = ldu + L_WO + ((wave & 3) * 12 * 64 + lane) * 4;
  const int sw = wave - NH;
  const size_t kvbase = (size_t)h * HEAD_BYTES;
  constexpr unsigned ROW1 = H * HEAD_BYTES;
  const __amdgpu_buffer_rsrc_t rk = __builtin_amdgcn_make_buffer_rsrc((void*)(kc + kvbase), 0, ROW1 + HEAD_BYTES, RSRC3);
  const unsigned kvo = r * (DH * 2) + c * 16;
  u32x4 buf[RPRE][3];
  unsigned so = (pq * NSW + sw) * TILE_BYTES + (h & 1) * RA * STRIDE;
  if (wave >= NH) {
    const __amdgpu_buffer_rsrc_t rv = __builtin_amdgcn_make_buffer_rsrc((void*)(vc + kvbase), 0, ROW1 + HEAD_BYTES, RSRC3);
    unsigned wb = 0;
    static_assert(RA + RPRE + 2 < WSTEPS && RPRE % 2 == 0 && RPRE >= 2, "no wrap before the main loop");
#pragma unroll
    for (int j = 0; j < RPRE; ++j) {
      if (j >= 2) { WAIT_VM3(); SB(); }
#pragma unroll
      for (int t = 0; t < 3; ++t) { buf[j][t] = LD128(rk, kvo + t * 128, so, AUX_NT); SB(); }
      so += STRIDE;
    }
    LOAD_SMALL();
    __syncthreads();
    u32x4 q[3];
#pragma unroll
    for (int t = 0; t < 3; ++t) q[t] = *reinterpret_cast<const u32x4*>(ldu + L_Q + (8 * t + c) * 4);
    float* sc = lds + L_SC + sw * 512 + r;

#define PIN12(A) asm volatile("" : "+v"(A[0]), "+v"(A[1]), "+v"(A[2]), "+v"(A[3]), "+v"(A[4]), "+v"(A[5]), "+v"(A[6]), "+v"(A[7]), \
    "+v"(A[8]), "+v"(A[9]), "+v"(A[10]), "+v"(A[11]) :: "memory")
#define ADVANCE() { so += STRIDE; if (so >= HEAD_BYTES) so -= HEAD_BYTES; }
#define KSTEP(B, REFILL, RS, SLOT) { \
    float s0_ = 0.f, s1_ = 0.f, s2_ = 0.f; \
    _Pragma("unroll") for (int j_ = 0; j_ < 4; ++j_) { s0_ = dot2(B[0][j_], q[0][j_], s0_); s1_ = dot2(B[1][j_], q[1][j_], s1_); s2_ = dot2(B[2][j_], q[2][j_], s2_); } \
    asm volatile("" : "+v"(s0_), "+v"(s1_), "+v"(s2_) :: "memory"); \
    SB(); \
    if (REFILL) { _Pragma("unroll") for (int u_ = 0; u_ < 3; ++u_) B[u_] = LD128(RS, kvo + u_ * 128, wb + so, AUX_NT); ADVANCE(); } \
    SB(); \
    float s_ = s0_ + s1_ + s2_; \
    s_ += dpp<XOR1>(s_); s_ += dpp<XOR2>(s_); s_ += dpp<HMIR>(s_); \
    s_ = rbf(rbf(s_) * SCALE); \
    if (c == 0) sc[(SLOT) * 8] = s_; \
    MEMBAR(); \
    SB(); }
#define VSTEP(B, REFILL, P) { \
    f32x2 vf_[12]; \
    _Pragma("unroll") for (int t_ = 0; t_ < 3; ++t_) \
      _Pragma("unroll") for (int j_ = 0; j_ < 4; ++j_) vf_[t_ * 4 + j_] = bf2(B[t_][j_]); \
    PIN12(vf_); \
    SB(); \
    if (REFILL) { _Pragma("unroll") for (int u_ = 0; u_ < 3; ++u_) B[u_] = LD128(rv, kvo + u_ * 128, wb + so, AUX_NT); ADVANCE(); } \
    SB(); \
    _Pragma("unroll") for (int k_ = 0; k_ < 12; ++k_) acc[k_] = fma2(splat(P), vf_[k_], acc[k_]); \
    PIN12(acc); \
    SB(); }

    KSTEP(buf[0], 1, rk, 0);
    KSTEP(buf[1], 1, rk, 1);
#pragma unroll
    for (int jb = 2; jb < RPRE; ++jb) KSTEP(buf[jb], 0, rk, jb);
    sc += RPRE * 8;
#pragma nounroll
    for (int i = RPRE; i < STEPS - 2; i += 2) {
      if (i == WSTEPS - 2) wb = ROW1;
      if (i == WSTEPS) {
#pragma unroll
        for (int t = 0; t < 3; ++t) q[t] = *reinterpret_cast<const u32x4*>(ldu + L_Q + 96 + (8 * t + c) * 4);
      }
      KSTEP(buf[0], 1, rk, 0);
      KSTEP(buf[1], 1, rk, 1);
      sc += 16;
    }
    wb = 0;
    KSTEP(buf[0], 1, rv, 0);
    KSTEP(buf[1], 1, rv, 1);
    {
      f32x4* const sp4 = reinterpret_cast<f32x4*>(lds + L_SC + sw * 512) + lane;
      f32x4 sa = sp4[0], sb = sp4[64];
      float ma = fmaxf(fmaxf(sa.x, sa.y), fmaxf(sa.z, sa.w)), mb = fmaxf(fmaxf(sb.x, sb.y), fmaxf(sb.z, sb.w));
      ma = fmaxf(ma, dpp<XOR1>(ma)); ma = fmaxf(ma, dpp<XOR2>(ma)); ma = fmaxf(ma, dpp<HMIR>(ma)); ma = fmaxf(ma, dpp<ROR8>(ma));
      mb = fmaxf(mb, dpp<XOR1>(mb)); mb = fmaxf(mb, dpp<XOR2>(mb)); mb = fmaxf(mb, dpp<HMIR>(mb)); mb = fmaxf(mb, dpp<ROR8>(mb));
      ma = max3216(ma); mb = max3216(mb);
#pragma unroll
      for (int k = 0; k < 4; ++k) {
        sa[k] = __builtin_amdgcn_exp2f((sa[k] - ma) * LOG2E);
        sb[k] = __builtin_amdgcn_exp2f((sb[k] - mb) * LOG2E);
      }
      sp4[0] = sa; sp4[64] = sb;
      float l = fold32((sa.x + sa.y) + (sa.z + sa.w), (sb.x + sb.y) + (sb.z + sb.w));
      l = row_sum16(fold16(l, l));
      if ((lane & 31) == 0) *reinterpret_cast<f32x2*>(lds + L_ML + (lane >> 5) * 8 + 2 * sw) = f32x2{(lane ? mb : ma) * LOG2E, l};
      MEMBAR();
    }
    f32x2 acc[12];
#pragma unroll
    for (int k = 0; k < 12; ++k) acc[k] = f32x2{0.f, 0.f};
    sc = lds + L_SC + sw * 512 + r;
#define FLUSH(BB, ARR) { \
      float* const red_ = lds + L_RED + ((BB) * NSW + sw) * DH + fidx; \
      _Pragma("unroll") for (int t_ = 0; t_ < 3; ++t_) { \
        const float z0 = fold32(acc[t_ * 4 + 0].x, acc[t_ * 4 + 2].x), z1 = fold32(acc[t_ * 4 + 0].y, acc[t_ * 4 + 2].y); \
        const float z2 = fold32(acc[t_ * 4 + 1].x, acc[t_ * 4 + 3].x), z3 = fold32(acc[t_ * 4 + 1].y, acc[t_ * 4 + 3].y); \
        red_[64 * t_] = fold8(fold16(z0, z2), fold16(z1, z3), bit3); \
      } \
      if (lane == 0) ARR = __hip_atomic_fetch_add(ldu + L_CNT + (BB), 1u, __ATOMIC_ACQ_REL, __HIP_MEMORY_SCOPE_WORKGROUP); }
#pragma nounroll
    for (int i = 0; i < STEPS - 2; i += 2) {
      const float p0 = sc[0], p1 = sc[8];
      sc += 16;
      if (i == WSTEPS - 2) wb = ROW1;
      SB();
      VSTEP(buf[0], 1, p0);
      VSTEP(buf[1], 1, p1);
      if (i == WSTEPS - 2) {
        unsigned unused = 0;
        FLUSH(0, unused);
        (void)unused;
#pragma unroll
        for (int k = 0; k < 12; ++k) acc[k] = f32x2{0.f, 0.f};
      }
    }
    LDS_WAIT(4, SETUP_DONE, 0);
#pragma unroll
    for (int i = 0; i < 6; ++i) {
      wop[i][0] = *reinterpret_cast<const u32x4*>(wl + (2 * i) * 256);
      wop[i][1] = *reinterpret_cast<const u32x4*>(wl + (2 * i + 1) * 256);
    }
    wv = *reinterpret_cast<const f32x4*>(lds + L_W + 16 + ((wave >> 1) & 1) * 8 + (lane >> 5) * 4);
    const f32x4 fw = *reinterpret_cast<const f32x4*>(lds + L_F);
    {
      const float p0 = sc[0], p1 = sc[8];
      MEMBAR();
      SB();
      VSTEP(buf[0], 0, p0);
      VSTEP(buf[1], 0, p1);
    }
    unsigned arrived = 0;
    FLUSH(1, arrived);
    if (__builtin_amdgcn_readfirstlane(arrived) == NSW - 1) {
      const int d4 = lane < 48 ? 4 * lane : 0;
      f32x4 v[NSW];
#pragma unroll
      for (int w = 0; w < NSW; ++w) v[w] = *reinterpret_cast<const f32x4*>(lds + L_RED + (NSW + w) * DH + d4);
      const f32x4 o = (v[0] * fw.x + v[1] * fw.y) + (v[2] * fw.z + v[3] * fw.w);
      ST128((u32x4{vbit(o.x), vbit(o.y), vbit(o.z), vbit(o.w)}), rw, lane < 48 ? lane * 16 : OOB, WS_P + bid * 2048 + 1024);
      MEMBAR();
      ldu[L_CNT + 5] = 1u;
    }
    LDS_WAIT(5, 1, 0);
  } else {
    if (wave < 3) {
      const __amdgpu_buffer_rsrc_t rq = __builtin_amdgcn_make_buffer_rsrc((void*)wq, 0, D * H * DH * 2, RSRC3);
      unsigned xs[2][5], xp[2];
#pragma unroll
      for (int bb = 0; bb < 2; ++bb) {
#pragma unroll
        for (int i = 0; i < 5; ++i) xs[bb][i] = __builtin_amdgcn_raw_buffer_load_b32(rx, lane * 4 + i * 256 + bb * D * 2, 0, 0);
        xp[bb] = __builtin_amdgcn_raw_buffer_load_b32(rx, (c < 5 ? (8 * c + r) * 4 : 0) + bb * D * 2, qp * 160, 0);
      }
      const u32x2 ep = __builtin_amdgcn_raw_buffer_load_b64(rw, vzero, WS_EPOCH, AUX_SC1);
      SB();
      u32x4 wqr[5][2];
      {
        const unsigned vo = (unsigned)(2 * r) * (H * DH * 2) + c * 16;
        const unsigned qso = (unsigned)(80 * qp) * (H * DH * 2) + qh * (DH * 2) + wave * 128;
#pragma unroll
        for (int i = 0; i < 5; ++i)
#pragma unroll
          for (int e = 0; e < 2; ++e) { wqr[i][e] = LD128(rq, vo, qso + (16 * i + e) * (H * DH * 2), AUX_NT); SB(); }
      }
      float s0 = 0.f, s1 = 0.f, q0 = 0.f, q1 = 0.f;
#pragma unroll
      for (int i = 0; i < 5; ++i) {
        s0 = dot2(xs[0][i], ONES, s0); q0 = dot2(xs[0][i], xs[0][i], q0);
        s1 = dot2(xs[1][i], ONES, s1); q1 = dot2(xs[1][i], xs[1][i], q1);
      }
      const float st = row_sum16(fold16(fold32(s0, s1), fold32(q0, q1)));
      auto sw2 = __builtin_amdgcn_permlane16_swap(__float_as_uint(st), __float_as_uint(st), false, false);
      const float mean = __uint_as_float(sw2[0]) * (1.0f / D);
      const float var = fmaxf(fmaf(-mean, mean, __uint_as_float(sw2[1]) * (1.0f / D)), 0.f);
      const float inv = ln_den_inv(var), mb = rbf(mean);
      const float m0 = rdlane(mb, 0), m1 = rdlane(mb, 32), i0 = rdlane(inv, 0), i1 = rdlane(inv, 32);
      const unsigned xn0 = pack_bf16(rbf(bflo(xp[0]) - m0) * i0, rbf(bfhi(xp[0]) - m0) * i0);
      const unsigned xn1 = pack_bf16(rbf(bflo(xp[1]) - m1) * i1, rbf(bfhi(xp[1]) - m1) * i1);
      const unsigned long long tg = mix64(((unsigned long long)(unsigned)__builtin_amdgcn_readfirstlane(ep.y) << 32) | (unsigned)__builtin_amdgcn_readfirstlane(ep.x));
      const unsigned thi = (unsigned)(tg >> 32), t8 = (unsigned)tg & 0xff000000u;
      float a0[8], a1q[8];
#pragma unroll
      for (int k = 0; k < 8; ++k) { a0[k] = 0.f; a1q[k] = 0.f; }
#define QGROUP(I) { \
        unsigned b0_ = __builtin_amdgcn_update_dpp(0u, xn0, 0x150 + I, 0xf, 0x3, false); b0_ = __builtin_amdgcn_update_dpp(b0_, xn0, 0x158 + I, 0xf, 0xc, false); \
        unsigned b1_ = __builtin_amdgcn_update_dpp(0u, xn1, 0x150 + I, 0xf, 0x3, false); b1_ = __builtin_amdgcn_update_dpp(b1_, xn1, 0x158 + I, 0xf, 0xc, false); \
        _Pragma("unroll") for (int j = 0; j < 4; ++j) { \
          const unsigned plo = __builtin_amdgcn_perm(wqr[I][1][j], wqr[I][0][j], 0x05040100u), phi = __builtin_amdgcn_perm(wqr[I][1][j], wqr[I][0][j], 0x07060302u); \
          a0[2 * j] = dot2(plo, b0_, a0[2 * j]); a0[2 * j + 1] = dot2(phi, b0_, a0[2 * j + 1]); \
          a1q[2 * j] = dot2(plo, b1_, a1q[2 * j]); a1q[2 * j + 1] = dot2(phi, b1_, a1q[2 * j + 1]); } }
      QGROUP(0) QGROUP(1) QGROUP(2) QGROUP(3) QGROUP(4)
      float z[8];
#pragma unroll
      for (int k = 0; k < 8; ++k) z[k] = fold32(a0[k], a1q[k]);
      float y[4];
#pragma unroll
      for (int k = 0; k < 4; ++k) y[k] = fold16(z[k], z[k + 4]);
      const float u0 = fold8(y[0], y[2], bit3), u1 = fold8(y[1], y[3], bit3);
      const unsigned pidx = 32 * wave + 4 * c + 2 * ((lane >> 4) & 1) + (bit3 ? 1 : 0);
      ST128(pack2(u0, u1, t8, thi), rw, ((lane >> 5) * 8 * 96 + pidx) * 16, WS_QP + (qh * 16 + qp) * (96 * 16));
      asm volatile("s_waitcnt vmcnt(0)" ::: "memory");
      {
        const unsigned qo = ((lane >> 5) * 8 * 96 + 32 * wave + (lane & 31)) * 16;
        const unsigned qb = WS_QP + h * 16 * (96 * 16);
        u32x4 gq[8];
        for (unsigned spins = 0;; ++spins) {
          MEMBAR();
#pragma unroll
          for (int k = 0; k < 8; ++k) gq[k] = LD128(rw, qo, qb + k * (96 * 16), AUX_SC1);
          unsigned bad = 0;
#pragma unroll
          for (int k = 0; k < 8; ++k) bad |= tag_bad(gq[k], t8, thi);
          if (__all(bad == 0u) || spins > SPIN_CAP) break;
        }
        const f32x2 qq = ((g01(gq[0]) + g01(gq[2])) + (g01(gq[4]) + g01(gq[6]))) + ((g01(gq[1]) + g01(gq[3])) + (g01(gq[5]) + g01(gq[7])));
        ldu[L_Q + (lane >> 5) * 96 + 32 * wave + (lane & 31)] = pack_bf16(qq.x, qq.y);
      }
      if (tid == 0) *reinterpret_cast<u32x2*>(ldu + L_TAG) = u32x2{(unsigned)tg, thi};
    }
    const unsigned yoff = ((lane >> 5) * 8 * 320 + (lane & 31)) * 16;
    const unsigned ybo = WS_Y + (team * 320 + pc * 32) * 16 + b * 8;
    if (wave == 3) {
      if (lane < 8) ldu[L_CNT + lane] = 0u;
      const u32x2 ep = LD64(rw, vzero, WS_EPOCH, AUX_SC1);
      ST128((u32x4{0u, 0u, 0u, 0u}), rw, lane * 16, WS_P + bid * 2048);
      ST128((u32x4{0u, 0u, 0u, 0u}), rw, lane * 16, WS_P + bid * 2048 + 1024);
      ST64((u32x2{0u, 0u}), rw, proj ? yoff : OOB, ybo);
      asm volatile("s_waitcnt vmcnt(0)" ::: "memory");
      const unsigned long long tg = mix64(((unsigned long long)(unsigned)__builtin_amdgcn_readfirstlane(ep.y) << 32) | (unsigned)__builtin_amdgcn_readfirstlane(ep.x));
      ST64((u32x2{(unsigned)tg, (unsigned)(tg >> 32)}), rw, lane == 0 ? 0u : OOB, WS_RDY + (team * 32 + jj) * 8);
    }
    __syncthreads();
    LOAD_SMALL();
    const __amdgpu_buffer_rsrc_t ro = __builtin_amdgcn_make_buffer_rsrc((void*)wo, 0, D * H * DH * 2, RSRC3);
    const unsigned vo = (unsigned)(2 * r) * (D * 2) + c * 16;
    const unsigned wso = !proj ? OOB : (unsigned)(768 * team + 384 * b + 96 * wave) * (D * 2) + pc * 128;
#pragma unroll
    for (int i = 0; i < 6; ++i) {
      {
        const unsigned slot = (unsigned)(__builtin_amdgcn_s_memrealtime() >> WOSLOT);
        for (int spins = 0; spins < 4096 && (unsigned)(__builtin_amdgcn_s_memrealtime() >> WOSLOT) == slot; ++spins) __builtin_amdgcn_s_sleep(1);
      }
      const u32x4 e0 = LD128(ro, vo, wso + 16 * i * (D * 2), AUX_NT);
      const u32x4 e1 = LD128(ro, vo + D * 2, wso + 16 * i * (D * 2), AUX_NT);
      u32x4 p0, p1;
      p0.x = __builtin_amdgcn_perm(e1.x, e0.x, 0x05040100u); p0.y = __builtin_amdgcn_perm(e1.x, e0.x, 0x07060302u);
      p0.z = __builtin_amdgcn_perm(e1.y, e0.y, 0x05040100u); p0.w = __builtin_amdgcn_perm(e1.y, e0.y, 0x07060302u);
      p1.x = __builtin_amdgcn_perm(e1.z, e0.z, 0x05040100u); p1.y = __builtin_amdgcn_perm(e1.z, e0.z, 0x07060302u);
      p1.z = __builtin_amdgcn_perm(e1.w, e0.w, 0x05040100u); p1.w = __builtin_amdgcn_perm(e1.w, e0.w, 0x07060302u);
      wop[i][0] = p0; wop[i][1] = p1;
      *reinterpret_cast<u32x4*>(wl + (2 * i) * 256) = p0;
      *reinterpret_cast<u32x4*>(wl + (2 * i + 1) * 256) = p1;
      MEMBAR();
      SB();
    }
    if (lane == 0) LDS_ARRIVE(4);
    if (wave == 0) {
      const u32x2 tg = *reinterpret_cast<const u32x2*>(ldu + L_TAG);
      for (unsigned spins = 0; spins < SPIN_CAP; ++spins) {
        MEMBAR();
        const u32x4 r0 = LD128(rw, lane * 16, WS_RDY, AUX_SC1), r1 = LD128(rw, lane * 16 + 1024, WS_RDY, AUX_SC1);
        const unsigned bad = (r0.x ^ tg.x) | (r0.y ^ tg.y) | (r0.z ^ tg.x) | (r0.w ^ tg.y) | (r1.x ^ tg.x) | (r1.y ^ tg.y) | (r1.z ^ tg.x) | (r1.w ^ tg.y);
        if (__all(bad == 0u)) break;
        __builtin_amdgcn_s_sleep(32);
      }
      if (lane == 0) { LDS_ARRIVE(6); LDS_ARRIVE(4); }
    }
    LDS_WAIT(0, NSW, 16);
    if (wave == 1) {
      const int d4 = lane < 48 ? 4 * lane : 0;
      const f32x4 ma = *reinterpret_cast<const f32x4*>(lds + L_ML + (lane >> 5) * 8), mc = *reinterpret_cast<const f32x4*>(lds + L_ML + (lane >> 5) * 8 + 4);
      f32x4 v[NSW];
#pragma unroll
      for (int w = 0; w < NSW; ++w) v[w] = *reinterpret_cast<const f32x4*>(lds + L_RED + w * DH + d4);
      const float M = __uint_as_float(vbit(fmaxf(fmaxf(ma.x, ma.z), fmaxf(mc.x, mc.z))));
      const f32x4 f = {__builtin_amdgcn_exp2f(ma.x - M), __builtin_amdgcn_exp2f(ma.z - M), __builtin_amdgcn_exp2f(mc.x - M), __builtin_amdgcn_exp2f(mc.z - M)};
      const float Ls = (ma.y * f.x + ma.w * f.y) + (mc.y * f.z + mc.w * f.w);
      if (lane == 32) *reinterpret_cast<f32x4*>(lds + L_F) = f;
      f32x4 o = (v[0] * rdlane(f.x, 0) + v[1] * rdlane(f.y, 0)) + (v[2] * rdlane(f.z, 0) + v[3] * rdlane(f.w, 0));
      const float M0 = rdlane(M, 0), L0 = rdlane(Ls, 0);
      if (lane >= 48) o = f32x4{M0, L0, M, Ls};
      ST128((u32x4{vbit(o.x), vbit(o.y), vbit(o.z), vbit(o.w)}), rw, lane <= 48 ? lane * 16 : OOB, WS_P + bid * 2048);
      if (lane == 0) LDS_ARRIVE(4);
    }
    __builtin_amdgcn_s_sleep(127);
    LDS_WAIT(6, 1, 16);
    if (proj) {
      const unsigned mbase = WS_P + (unsigned)(team + 64 * (2 * b + (wave >> 1))) * 2048 + 48 * 16;
      u32x2 gm;
      for (unsigned spins = 0; spins < SPIN_CAP; ++spins) {
        MEMBAR();
        gm = LD64(rw, (lane & 7) * (8 * 2048) + (lane & 8), mbase, AUX_SC1);
        if (__all((gm.x & gm.y & 1u) != 0u)) break;
        __builtin_amdgcn_s_sleep(32);
      }
      const float ms = __uint_as_float(gm.x);
      float M = fmaxf(ms, dpp<XOR1>(ms)); M = fmaxf(M, dpp<XOR2>(M)); M = fmaxf(M, dpp<HMIR>(M));
      const float f = __builtin_amdgcn_exp2f(ms - M);
      float Ls = f * __uint_as_float(gm.y);
      Ls += dpp<XOR1>(Ls); Ls += dpp<XOR2>(Ls); Ls += dpp<HMIR>(Ls);
      if (lane < 16) lds[L_W + (lane >> 3) * 16 + (wave >> 1) * 8 + (lane & 1) * 4 + ((lane >> 1) & 3)] = f * __builtin_amdgcn_rcpf(Ls);
      MEMBAR();
      wv = *reinterpret_cast<const f32x4*>(lds + L_W + (wave >> 1) * 8 + (lane >> 5) * 4);
    }
    if (lane == 0) LDS_ARRIVE(4);
  }
  const u32x2 tg2 = *reinterpret_cast<const u32x2*>(ldu + L_TAG);
  const unsigned tlo = __builtin_amdgcn_readfirstlane(tg2.x), thi = __builtin_amdgcn_readfirstlane(tg2.y);

  if (proj) {
    const unsigned pbase = WS_P + (unsigned)(team + 64 * (2 * b + ((wave >> 1) & 1))) * 2048 + (wave >> 2) * 1024 + (wave & 1) * 384;
    const int j = lane & 31;
    const unsigned po = (lane >> 5) * (8 * 2048) + (j < 24 ? j : 0) * 16;
    u32x4 go[4];
    unsigned pend = 0xfu, spins = 0;
    do {
      MEMBAR();
#pragma unroll
      for (int k = 0; k < 4; ++k)
        if (pend & (1u << k)) go[k] = LD128(rw, po, pbase + k * (16 * 2048), AUX_SC1);
#pragma unroll
      for (int k = 0; k < 4; ++k)
        if (pend & (1u << k)) { if (__all((go[k].x & go[k].y & go[k].z & go[k].w & 1u) != 0u)) pend &= ~(1u << k); }
      if (wave < NH && pend != 0u) __builtin_amdgcn_s_sleep(32);
    } while (pend != 0u && ++spins < SPIN_CAP);
    const f32x4 o = (__builtin_bit_cast(f32x4, go[0]) * wv.x + __builtin_bit_cast(f32x4, go[1]) * wv.y) +
                    (__builtin_bit_cast(f32x4, go[2]) * wv.z + __builtin_bit_cast(f32x4, go[3]) * wv.w);
    const unsigned pr = pack_bf16(fold32(o.x, o.z), fold32(o.y, o.w));
    if (j < 24) ldu[L_O + 48 * wave + 2 * j + (lane >> 5)] = pr;
    MEMBAR();
  }
  if (proj) {
    unsigned op[6];
#pragma unroll
    for (int i = 0; i < 6; ++i) op[i] = ldu[L_O + 48 * wave + 8 * i + r];
    float y[8];
#pragma unroll
    for (int k = 0; k < 8; ++k) y[k] = 0.f;
#pragma unroll
    for (int i = 0; i < 6; ++i) {
#pragma unroll
      for (int k = 0; k < 4; ++k) { y[k] = dot2(wop[i][0][k], op[i], y[k]); y[k + 4] = dot2(wop[i][1][k], op[i], y[k + 4]); }
    }
    const float z0 = fold32(y[0], y[4]), z1 = fold32(y[1], y[5]), z2 = fold32(y[2], y[6]), z3 = fold32(y[3], y[7]);
    const float u = fold8(fold16(z0, z2), fold16(z1, z3), bit3);
    lds[L_YR + wave * 64 + fidx] = u;
    const int bb = wave >> 2;
    unsigned arrived = 0;
    if (lane == 0) arrived = LDS_ARRIVE(2 + bb);
    if (__builtin_amdgcn_readfirstlane(arrived) == 3) {
      const int t = lane & 31;
      f32x2 v[4];
#pragma unroll
      for (int w = 0; w < 4; ++w) v[w] = *reinterpret_cast<const f32x2*>(lds + L_YR + (4 * bb + w) * 64 + 2 * t);
      const f32x2 ys = (v[0] + v[1]) + (v[2] + v[3]);
      ST64((u32x2{vbit(ys.x), vbit(ys.y)}), rw, lane < 32 ? lane * 16 : OOB, WS_Y + ((bb * 8 + team) * 320 + pc * 32) * 16 + b * 8);
    }
  }
  __syncthreads();
  CONV_SMALL();
  if (wave >= 3) {
    u32x4 gy[8];
    const unsigned yo = (tid - 192) * 16, ybase = WS_Y + b * (8 * 320 * 16);
    unsigned pend = 0xffu, spins = 0;
    do {
      MEMBAR();
#pragma unroll
      for (int s = 0; s < 8; ++s)
        if (pend & (1u << s)) gy[s] = LD128(rw, yo, ybase + s * (320 * 16), AUX_SC1);
#pragma unroll
      for (int s = 0; s < 8; ++s)
        if (pend & (1u << s)) { if (__all((gy[s].x & gy[s].y & gy[s].z & gy[s].w & 1u) != 0u)) pend &= ~(1u << s); }
    } while (pend != 0u && ++spins < SPIN_CAP);
    f32x2 ap[8];
#pragma unroll
    for (int s = 0; s < 8; ++s) ap[s] = asf2(gy[s].x, gy[s].y) + asf2(gy[s].z, gy[s].w);
    const f32x2 a = ((ap[0] + ap[1]) + (ap[2] + ap[3])) + ((ap[4] + ap[5]) + (ap[6] + ap[7]));
    const unsigned hp = pack_bf16(bflo(xres) + rbf(a.x), bfhi(xres) + rbf(a.y));
    ldu[L_H + tid - 192] = hp;
    float st = fold32(dot2(hp, ONES, 0.f), dot2(hp, hp, 0.f));
    st = row_sum16(fold16(st, st));
    if ((lane & 31) == 0) lds[L_ST + (wave - 3) * 2 + (lane >> 5)] = st;
  }
  __syncthreads();
  const int d0 = 5 * (team * 16 + cb);
  float he;
  {
    const int de = d0 + (lane < 5 ? lane : 0);
    const unsigned hw = ldu[L_H + (de >> 1)];
    f32x2 sv[5];
#pragma unroll
    for (int w = 0; w < 5; ++w) sv[w] = *reinterpret_cast<const f32x2*>(lds + L_ST + 2 * w);
    const f32x2 st = (((sv[0] + sv[1]) + (sv[2] + sv[3])) + sv[4]) * splat(1.0f / D);
    he = (de & 1) ? bfhi(hw) : bflo(hw);
    const float mean1 = st.x;
    const float var1 = fmaxf(fmaf(-mean1, mean1, st.y), 0.f);
    const float i1 = ln_den_inv(var1), m1 = rbf(mean1);
    const float m2 = rbf((mean1 - m1) * i1), i2 = ln_den_inv(var1 * i1 * i1);
    const float tl = rbf(rbf(rbf(rbf(he - m1) * i1) - m2) * i2);
    constexpr float G1 = -2.0f * 0.7978845608028654f * LOG2E, G3 = G1 * 0.044715f;
    float te[5];
#pragma unroll
    for (int e = 0; e < 5; ++e) te[e] = rdlane(tl, e);
    f32x2 u[8], z[8];
#pragma unroll
    for (int e = 0; e < 5; ++e) u[e] = fma2(splat(te[e]), fw1, fb1);
    u[5] = fma2(f32x2{te[0], te[1]}, splat(gw1), splat(gb1));
    u[6] = fma2(f32x2{te[2], te[3]}, splat(gw1), splat(gb1));
    u[7] = fma2(splat(te[4]), splat(gw1), splat(gb1));
    SB();
#pragma unroll
    for (int k = 0; k < 8; ++k) z[k] = u[k] * u[k];
    SB();
#pragma unroll
    for (int k = 0; k < 8; ++k) z[k] = fma2(z[k], splat(G3), splat(G1));
    SB();
#pragma unroll
    for (int k = 0; k < 8; ++k) z[k] = z[k] * u[k];
    SB();
#pragma unroll
    for (int k = 0; k < 8; ++k) { z[k].x = __builtin_amdgcn_exp2f(z[k].x); if (k < 7) z[k].y = __builtin_amdgcn_exp2f(z[k].y); }
#pragma unroll
    for (int k = 0; k < 8; ++k) u[k] = u[k] * (k < 5 ? fw2 : splat(gw2));
    SB();
#pragma unroll
    for (int k = 0; k < 8; ++k) z[k] = z[k] + splat(1.0f);
    SB();
#pragma unroll
    for (int k = 0; k < 8; ++k) { z[k].x = __builtin_amdgcn_rcpf(z[k].x); if (k < 7) z[k].y = __builtin_amdgcn_rcpf(z[k].y); }
    SB();
#pragma unroll
    for (int k = 0; k < 8; ++k) u[k] = u[k] * z[k];
    const float ya0 = (u[0].x + u[0].y) + u[5].x, ya1 = (u[1].x + u[1].y) + u[5].y, ya2 = (u[2].x + u[2].y) + u[6].x,
                ya3 = (u[3].x + u[3].y) + u[6].y, ya4 = (u[4].x + u[4].y) + u[7].x;
    const float f4 = fold32(ya4, ya4);
    float r0 = fold8(fold16(fold32(ya0, ya1), fold32(ya2, ya3)), fold16(f4, f4), bit3);
    r0 += dpp<XOR1>(r0); r0 += dpp<XOR2>(r0); r0 += dpp<HMIR>(r0);
    const int el = bit3 ? 4 : (lane >> 5) + 2 * ((lane >> 4) & 1);
    if ((lane & 15) == 0 || lane == 8) lds[L_MR + el * 8 + wave] = r0;
  }
  __syncthreads();
  if (tid < 5) {
    const f32x4 pa = *reinterpret_cast<const f32x4*>(lds + L_MR + tid * 8), pb = *reinterpret_cast<const f32x4*>(lds + L_MR + tid * 8 + 4);
    const f32x4 ps = pa + pb;
    const float yv = (ps.x + ps.y) + (ps.z + ps.w);
    const unsigned o = pack_bf16(he + rbf(rbf(yv) + fb2), 0.f);
    __builtin_amdgcn_raw_buffer_store_b16((unsigned short)o, __builtin_amdgcn_make_buffer_rsrc((void*)out, 0, 2 * D * 2 + 4, RSRC3),
                                          tid * 2, (b * D + d0) * 2, 0);
  }
  if (tid == 0 && bid == 0)
    __builtin_amdgcn_raw_buffer_store_b64(u32x2{tlo, thi}, rw, 0, WS_EPOCH, AUX_SC1);
}

}

extern "C" void kernel_launch(void* const* d_in, const int* in_sizes, int n_in, void* d_out, int out_size, void* d_ws,
                              size_t ws_size, hipStream_t stream) {
  (void)in_sizes; (void)n_in; (void)out_size; (void)ws_size;
  fused_kernel<<<dim3(NWG), dim3(NTHR), 0, stream>>>(
      (const char*)d_in[0], (const char*)d_in[1], (const char*)d_in[2], (const char*)d_in[3], (const char*)d_in[4],
      (const char*)d_in[6], (const char*)d_in[7], (const char*)d_in[8], (const char*)d_in[9], (char*)d_out, (char*)d_ws);
}
```

```cpp
#include <hip/hip_runtime.h>
#include <hip/hip_bf16.h>
#include <stdint.h>

namespace {

typedef unsigned u32x4 __attribute__((ext_vector_type(4)));
typedef unsigned u32x2 __attribute__((ext_vector_type(2)));
typedef float f32x2 __attribute__((ext_vector_type(2)));
typedef float f32x4 __attribute__((ext_vector_type(4)));
typedef __bf16 bf16x2 __attribute__((ext_vector_type(2)));

constexpr int D = 640, H = 32, SK = 8192, DH = 192, FF = 1536;
constexpr int NWG = 256, NTHR = 512, NWAVE = 8;
constexpr unsigned TILE_BYTES = 8 * DH * 2;
constexpr unsigned HEAD_BYTES = SK * DH * 2;
constexpr int RA = 8;
constexpr int RPRE = 8;
constexpr int EB = 4;
constexpr int WOSLOT = 9;
constexpr int GS = 8;
constexpr int NSW = 4, NH = NWAVE - NSW;
constexpr int WSTEPS = SK / 8 / (GS * NSW);
constexpr int STEPS = 2 * WSTEPS;
constexpr unsigned STRIDE = GS * NSW * TILE_BYTES;
constexpr int AUX_NT = 2, AUX_SC1 = 16;
constexpr unsigned RSRC3 = 0x00020000u;
constexpr unsigned OOB = 0x80000000u;

constexpr unsigned WS_EPOCH = 0;
constexpr unsigned WS_RDY = 4096;
constexpr unsigned WS_P = 8192;
constexpr unsigned WS_Y = WS_P + NWG * 2048;
constexpr unsigned WS_QP = WS_Y + 2 * 8 * 320 * 16;
constexpr unsigned WS_END = WS_QP + 32 * 2 * 8 * 96 * 16;

constexpr int L_RED = 0;
constexpr int L_ML = L_RED + 2 * NSW * DH;
constexpr int L_Q = L_ML + 16;
constexpr int L_TAG = L_Q + 192;
constexpr int L_CNT = L_TAG + 4;
constexpr int L_W = L_CNT + 8;
constexpr int L_F = L_W + 32;
constexpr int L_O = L_F + 4;
constexpr int L_YR = L_O + 384;
constexpr int L_H = L_YR + 512;
constexpr int L_ST = L_H + 320;
constexpr int L_MR = L_ST + 16;
constexpr int L_DUM = L_MR + 64;
constexpr int L_SC = L_DUM + 64;
constexpr int L_WO = L_SC + NSW * 512;
constexpr int L_TOTAL = 24 * 1024;
static_assert(L_WO + 24 * 2 * 64 * 4 <= L_TOTAL, "lds");
static_assert(L_WO % 4 == 0 && L_Q % 4 == 0 && L_ST % 4 == 0 && L_MR % 4 == 0, "alignment");

__device__ __forceinline__ float dot2(unsigned a, unsigned b, float c) {
  return __builtin_amdgcn_fdot2_f32_bf16(__builtin_bit_cast(bf16x2, a), __builtin_bit_cast(bf16x2, b), c, false);
}
template <int CTRL> __device__ __forceinline__ float dpp(float x) {
  return __builtin_bit_cast(float, __builtin_amdgcn_mov_dpp(__builtin_bit_cast(int, x), CTRL, 0xf, 0xf, true));
}
constexpr int XOR1 = 0xB1, XOR2 = 0x4E, HMIR = 0x141, ROR8 = 0x128;
constexpr unsigned ONES = 0x3f803f80u;
__device__ __forceinline__ float bflo(unsigned u) { return __builtin_bit_cast(float, u << 16); }
__device__ __forceinline__ float bfhi(unsigned u) { return __builtin_bit_cast(float, u & 0xffff0000u); }
__device__ __forceinline__ f32x2 bf2(unsigned u) { return f32x2{bflo(u), bfhi(u)}; }
__device__ __forceinline__ unsigned pack_bf16(float lo, float hi) {
  bf16x2 v; v[0] = (__bf16)lo; v[1] = (__bf16)hi;
  return __builtin_bit_cast(unsigned, v);
}
__device__ __forceinline__ float rbf(float x) {
  typedef float f32x2_ __attribute__((ext_vector_type(2)));
  return __builtin_bit_cast(float, __builtin_convertvector(f32x2_{0.f, x}, bf16x2));
}
__device__ __forceinline__ float fold32(float a, float b) {
  auto r = __builtin_amdgcn_permlane32_swap(__float_as_uint(a), __float_as_uint(b), false, false);
  return __uint_as_float(r[0]) + __uint_as_float(r[1]);
}
__device__ __forceinline__ float fold16(float a, float b) {
  auto r = __builtin_amdgcn_permlane16_swap(__float_as_uint(a), __float_as_uint(b), false, false);
  return __uint_as_float(r[0]) + __uint_as_float(r[1]);
}
__device__ __forceinline__ float fold8(float a, float b, bool bit3) {
  const float keep = bit3 ? b : a, send = bit3 ? a : b;
  return keep + dpp<ROR8>(send);
}
__device__ __forceinline__ float max3216(float x) {
  auto s = __builtin_amdgcn_permlane16_swap(__float_as_uint(x), __float_as_uint(x), false, false);
  x = fmaxf(__uint_as_float(s[0]), __uint_as_float(s[1]));
  auto t = __builtin_amdgcn_permlane32_swap(__float_as_uint(x), __float_as_uint(x), false, false);
  return fmaxf(__uint_as_float(t[0]), __uint_as_float(t[1]));
}
__device__ __forceinline__ float row_sum16(float x) {
  x += dpp<XOR1>(x); x += dpp<XOR2>(x); x += dpp<HMIR>(x); x += dpp<ROR8>(x);
  return x;
}
__device__ __forceinline__ unsigned long long mix64(unsigned long long z) {
  z += 0x9E3779B97F4A7C15ull;
  z = (z ^ (z >> 30)) * 0xBF58476D1CE4E5B9ull;
  z = (z ^ (z >> 27)) * 0x94D049BB133111EBull;
  return z ^ (z >> 31);
}
__device__ __forceinline__ u32x4 pack2(float a, float b, unsigned t8, unsigned thi) {
  u32x4 r; r.x = (__float_as_uint(a) >> 8) | t8; r.y = thi; r.z = (__float_as_uint(b) >> 8) | t8; r.w = thi; return r;
}
__device__ __forceinline__ unsigned tag_bad(u32x4 g, unsigned t8, unsigned thi) {
  return (((g.x ^ t8) | (g.z ^ t8)) >> 24) | (g.y ^ thi) | (g.w ^ thi);
}
__device__ __forceinline__ float g0(u32x4 g) { return __uint_as_float(g.x << 8); }
__device__ __forceinline__ float g1(u32x4 g) { return __uint_as_float(g.z << 8); }
__device__ __forceinline__ f32x2 g01(u32x4 g) { return f32x2{g0(g), g1(g)}; }
__device__ __forceinline__ unsigned vbit(float x) { return __float_as_uint(x) | 1u; }
__device__ __forceinline__ f32x2 asf2(unsigned a, unsigned b) { return f32x2{__uint_as_float(a), __uint_as_float(b)}; }
__device__ __forceinline__ float ln_den_inv(float var) {
  return __builtin_amdgcn_rcpf(rbf(__builtin_amdgcn_sqrtf(rbf(rbf(var) + 1e-5f))));
}
__device__ __forceinline__ float rdlane(float x, int l) { return __uint_as_float(__builtin_amdgcn_readlane(__float_as_uint(x), l)); }
__device__ __forceinline__ f32x2 splat(float x) { return f32x2{x, x}; }
__device__ __forceinline__ f32x2 fma2(f32x2 a, f32x2 b, f32x2 c) { return __builtin_elementwise_fma(a, b, c); }

#define SB() __builtin_amdgcn_sched_barrier(0)
#define MEMBAR() asm volatile("" ::: "memory")
#define LD128(rs, vo, so, aux) __builtin_amdgcn_raw_buffer_load_b128((rs), (vo), (so), (aux))
#define ST128(v, rs, vo, so) __builtin_amdgcn_raw_buffer_store_b128((v), (rs), (vo) + (so), 0, AUX_SC1)
#define LD64(rs, vo, so, aux) __builtin_amdgcn_raw_buffer_load_b64((rs), (vo), (so), (aux))
#define ST64(v, rs, vo, so) __builtin_amdgcn_raw_buffer_store_b64((v), (rs), (vo), (so), AUX_SC1)
#define WAIT_VM3() __builtin_amdgcn_s_waitcnt(0x0F73)

constexpr unsigned SPIN_CAP = 1u << 17;

__global__ void __launch_bounds__(NTHR, 2)
fused_kernel(const char* x, const char* kc, const char* vc, const char* wq, const char* wo, const char* w1,
             const char* b1, const char* w2, const char* b2, char* out, char* ws) {
  __shared__ __attribute__((aligned(16))) float lds[L_TOTAL];
  unsigned* const ldu = reinterpret_cast<unsigned*>(lds);
  const int tid = threadIdx.x, lane = tid & 63, wave = __builtin_amdgcn_readfirstlane(tid >> 6);
  const int bid = blockIdx.x, team = bid & 7, jj = bid >> 3, hl = jj >> 3, b = (jj >> 2) & 1, sp = jj & 3;
  const int h = team * 4 + hl, cb = hl * 4 + sp, pq = jj & 7;
  const int pc = sp * 4 + hl;
  const bool proj = pc < 10;
  const int qh = jj, qp = team;
  const int r = lane >> 3, c = lane & 7;
  const bool bit3 = (lane & 8) != 0;
  const int fidx = 8 * c + 4 * (lane >> 5) + 2 * ((lane >> 4) & 1) + (bit3 ? 1 : 0);
  asm volatile("" : "+s"(x), "+s"(kc), "+s"(vc), "+s"(wq), "+s"(wo), "+s"(w1), "+s"(b1), "+s"(w2), "+s"(b2), "+s"(out), "+s"(ws));
  const __amdgpu_buffer_rsrc_t rw = __builtin_amdgcn_make_buffer_rsrc((void*)ws, 0, WS_END, RSRC3);
  const __amdgpu_buffer_rsrc_t rx = __builtin_amdgcn_make_buffer_rsrc((void*)x, 0, 2 * D * 2, RSRC3);
  constexpr float SCALE = 0.07216878364870322f, LOG2E = 1.4426950408889634f;
  unsigned vzero;
  asm volatile("v_mov_b32 %0, 0" : "=v"(vzero));

  f32x2 fw1, fb1, fw2; float gw1, gb1, gw2, fb2;
  unsigned xres;
  unsigned short a1[3], a2[3], a3[3], a4;
#define LOAD_SMALL() { \
    const __amdgpu_buffer_rsrc_t r1 = __builtin_amdgcn_make_buffer_rsrc((void*)w1, 0, FF * 2 + 4, RSRC3); \
    const __amdgpu_buffer_rsrc_t r2 = __builtin_amdgcn_make_buffer_rsrc((void*)b1, 0, FF * 2 + 4, RSRC3); \
    const __amdgpu_buffer_rsrc_t r3 = __builtin_amdgcn_make_buffer_rsrc((void*)w2, 0, FF * 2 + 4, RSRC3); \
    _Pragma("unroll") for (int k = 0; k < 3; ++k) { \
      a1[k] = __builtin_amdgcn_raw_buffer_load_b16(r1, tid * 2 + 1024 * k, 0, 0); \
      a2[k] = __builtin_amdgcn_raw_buffer_load_b16(r2, tid * 2 + 1024 * k, 0, 0); \
      a3[k] = __builtin_amdgcn_raw_buffer_load_b16(r3, tid * 2 + 1024 * k, 0, 0); \
    } \
    a4 = __builtin_amdgcn_raw_buffer_load_b16(__builtin_amdgcn_make_buffer_rsrc((void*)b2, 0, 4, RSRC3), vzero, 0, 0); \
    xres = __builtin_amdgcn_raw_buffer_load_b32(rx, tid >= 192 ? (tid - 192) * 4 : OOB, b * D * 2, 0); \
    MEMBAR(); }
#define CONV_SMALL() { \
    fw1 = f32x2{bflo(a1[0]), bflo(a1[1])}; fb1 = f32x2{bflo(a2[0]), bflo(a2[1])}; fw2 = f32x2{bflo(a3[0]), bflo(a3[1])}; \
    gw1 = bflo(a1[2]); gb1 = bflo(a2[2]); gw2 = bflo(a3[2]); fb2 = bflo(a4); \
    asm volatile("" : "+v"(fw1), "+v"(fb1), "+v"(fw2), "+v"(gw1), "+v"(gb1), "+v"(gw2), "+v"(fb2), "+v"(xres) :: "memory"); }

#define LDS_WAIT(IDX, VAL, SLEEP) { unsigned spins_ = 0; \
    _Pragma("nounroll") while (__hip_atomic_load(ldu + L_CNT + (IDX), __ATOMIC_ACQUIRE, __HIP_MEMORY_SCOPE_WORKGROUP) != (unsigned)(VAL) && ++spins_ < (1u << 22)) \
      { if (SLEEP) __builtin_amdgcn_s_sleep(SLEEP); } }
#define LDS_ARRIVE_FAST(IDX) __hip_atomic_fetch_add(ldu + (lane == 0 ? L_CNT + (IDX) : L_DUM + lane), 1u, __ATOMIC_RELAXED, __HIP_MEMORY_SCOPE_WORKGROUP)
#define LDS_ARRIVE(IDX) __hip_atomic_fetch_add(ldu + L_CNT + (IDX), 1u, __ATOMIC_ACQ_REL, __HIP_MEMORY_SCOPE_WORKGROUP)
  constexpr int SETUP_DONE = 2 * NH + 2;
  u32x4 wop[6][2];
  f32x4 wv;
  unsigned* const wl = ldu + L_WO + ((wave & 3) * 12 * 64 + lane) * 4;
  const int sw = wave - NH;
  const size_t kvbase = (size_t)h * HEAD_BYTES;
  constexpr unsigned ROW1 = H * HEAD_BYTES;
  const __amdgpu_buffer_rsrc_t rk = __builtin_amdgcn_make_buffer_rsrc((void*)(kc + kvbase), 0, ROW1 + HEAD_BYTES, RSRC3);
  const unsigned kvo = r * (DH * 2) + c * 16;
  u32x4 buf[RPRE][3];
  unsigned so = (pq * NSW + sw) * TILE_BYTES + (h & 1) * RA * STRIDE;
  if (wave >= NH) {
    const __amdgpu_buffer_rsrc_t rv = __builtin_amdgcn_make_buffer_rsrc((void*)(vc + kvbase), 0, ROW1 + HEAD_BYTES, RSRC3);
    unsigned wb = 0;
    static_assert(RA + RPRE + 2 < WSTEPS && RPRE % 2 == 0 && RPRE >= 2, "no wrap before the main loop");
#pragma unroll
    for (int j = 0; j < RPRE; ++j) {
      if (j >= 2) { WAIT_VM3(); SB(); }
#pragma unroll
      for (int t = 0; t < 3; ++t) { buf[j][t] = LD128(rk, kvo + t * 128, so, AUX_NT); SB(); }
      so += STRIDE;
    }
    LOAD_SMALL();
    __syncthreads();
    u32x4 q[3];
#pragma unroll
    for (int t = 0; t < 3; ++t) q[t] = *reinterpret_cast<const u32x4*>(ldu + L_Q + (8 * t + c) * 4);
    float* sc = lds + L_SC + sw * 512 + r;

#define PIN12(A) asm volatile("" : "+v"(A[0]), "+v"(A[1]), "+v"(A[2]), "+v"(A[3]), "+v"(A[4]), "+v"(A[5]), "+v"(A[6]), "+v"(A[7]), \
    "+v"(A[8]), "+v"(A[9]), "+v"(A[10]), "+v"(A[11]) :: "memory")
#define ADVANCE() { so += STRIDE; if (so >= HEAD_BYTES) so -= HEAD_BYTES; }
#define KSTEP(B, REFILL, RS, SLOT) { \
    float s0_ = 0.f, s1_ = 0.f, s2_ = 0.f; \
    _Pragma("unroll") for (int j_ = 0; j_ < 4; ++j_) { s0_ = dot2(B[0][j_], q[0][j_], s0_); s1_ = dot2(B[1][j_], q[1][j_], s1_); s2_ = dot2(B[2][j_], q[2][j_], s2_); } \
    asm volatile("" : "+v"(s0_), "+v"(s1_), "+v"(s2_) :: "memory"); \
    SB(); \
    if (REFILL) { _Pragma("unroll") for (int u_ = 0; u_ < 3; ++u_) B[u_] = LD128(RS, kvo + u_ * 128, wb + so, AUX_NT); ADVANCE(); } \
    SB(); \
    float s_ = s0_ + s1_ + s2_; \
    s_ += dpp<XOR1>(s_); s_ += dpp<XOR2>(s_); s_ += dpp<HMIR>(s_); \
    s_ = rbf(rbf(s_) * SCALE); \
    if (c == 0) sc[(SLOT) * 8] = s_; \
    MEMBAR(); \
    SB(); }
#define VSTEP(B, REFILL, P) { \
    f32x2 vf_[12]; \
    _Pragma("unroll") for (int t_ = 0; t_ < 3; ++t_) \
      _Pragma("unroll") for (int j_ = 0; j_ < 4; ++j_) vf_[t_ * 4 + j_] = bf2(B[t_][j_]); \
    PIN12(vf_); \
    SB(); \
    if (REFILL) { _Pragma("unroll") for (int u_ = 0; u_ < 3; ++u_) B[u_] = LD128(rv, kvo + u_ * 128, wb + so, AUX_NT); ADVANCE(); } \
    SB(); \
    _Pragma("unroll") for (int k_ = 0; k_ < 12; ++k_) acc[k_] = fma2(splat(P), vf_[k_], acc[k_]); \
    PIN12(acc); \
    SB(); }

    KSTEP(buf[0], 1, rk, 0);
    KSTEP(buf[1], 1, rk, 1);
#pragma unroll
    for (int jb = 2; jb < RPRE; ++jb) KSTEP(buf[jb], 0, rk, jb);
    sc += RPRE * 8;
#pragma nounroll
    for (int i = RPRE; i < STEPS - 2; i += 2) {
      if (i == WSTEPS - 2) wb = ROW1;
      if (i == WSTEPS) {
#pragma unroll
        for (int t = 0; t < 3; ++t) q[t] = *reinterpret_cast<const u32x4*>(ldu + L_Q + 96 + (8 * t + c) * 4);
      }
      KSTEP(buf[0], 1, rk, 0);
      KSTEP(buf[1], 1, rk, 1);
      sc += 16;
    }
    wb = 0;
    KSTEP(buf[0], 1, rv, 0);
    KSTEP(buf[1], 1, rv, 1);
    {
      f32x4* const sp4 = reinterpret_cast<f32x4*>(lds + L_SC + sw * 512) + lane;
      f32x4 sa = sp4[0], sb = sp4[64];
      float ma = fmaxf(fmaxf(sa.x, sa.y), fmaxf(sa.z, sa.w)), mb = fmaxf(fmaxf(sb.x, sb.y), fmaxf(sb.z, sb.w));
      ma = fmaxf(ma, dpp<XOR1>(ma)); ma = fmaxf(ma, dpp<XOR2>(ma)); ma = fmaxf(ma, dpp<HMIR>(ma)); ma = fmaxf(ma, dpp<ROR8>(ma));
      mb = fmaxf(mb, dpp<XOR1>(mb)); mb = fmaxf(mb, dpp<XOR2>(mb)); mb = fmaxf(mb, dpp<HMIR>(mb)); mb = fmaxf(mb, dpp<ROR8>(mb));
      ma = max3216(ma); mb = max3216(mb);
#pragma unroll
      for (int k = 0; k < 4; ++k) {
        sa[k] = __builtin_amdgcn_exp2f((sa[k] - ma) * LOG2E);
        sb[k] = __builtin_amdgcn_exp2f((sb[k] - mb) * LOG2E);
      }
      sp4[0] = sa; sp4[64] = sb;
      float l = fold32((sa.x + sa.y) + (sa.z + sa.w), (sb.x + sb.y) + (sb.z + sb.w));
      l = row_sum16(fold16(l, l));
      if ((lane & 31) == 0) *reinterpret_cast<f32x2*>(lds + L_ML + (lane >> 5) * 8 + 2 * sw) = f32x2{(lane ? mb : ma) * LOG2E, l};
      MEMBAR();
    }
    f32x2 acc[12];
#pragma unroll
    for (int k = 0; k < 12; ++k) acc[k] = f32x2{0.f, 0.f};
    sc = lds + L_SC + sw * 512 + r;
#define FLUSH(BB, ARR) { \
      float* const red_ = lds + L_RED + ((BB) * NSW + sw) * DH + fidx; \
      _Pragma("unroll") for (int t_ = 0; t_ < 3; ++t_) { \
        const float z0 = fold32(acc[t_ * 4 + 0].x, acc[t_ * 4 + 2].x), z1 = fold32(acc[t_ * 4 + 0].y, acc[t_ * 4 + 2].y); \
        const float z2 = fold32(acc[t_ * 4 + 1].x, acc[t_ * 4 + 3].x), z3 = fold32(acc[t_ * 4 + 1].y, acc[t_ * 4 + 3].y); \
        red_[64 * t_] = fold8(fold16(z0, z2), fold16(z1, z3), bit3); \
      } \
      MEMBAR(); \
      ARR = LDS_ARRIVE_FAST(BB); \
      MEMBAR(); }
#pragma nounroll
    for (int i = 0; i < STEPS - 2 - EB; i += 2) {
      const float p0 = sc[0], p1 = sc[8];
      sc += 16;
      if (i == WSTEPS - 2) wb = ROW1;
      SB();
      VSTEP(buf[0], 1, p0);
      VSTEP(buf[1], 1, p1);
      if (i == WSTEPS - 2) {
        unsigned unused = 0;
        FLUSH(0, unused);
        (void)unused;
#pragma unroll
        for (int k = 0; k < 12; ++k) acc[k] = f32x2{0.f, 0.f};
      }
    }
#pragma unroll
    for (int jb = 2; jb < 2 + EB; ++jb) {
#pragma unroll
      for (int t = 0; t < 3; ++t) buf[jb][t] = LD128(rv, kvo + t * 128, wb + so, AUX_NT);
      ADVANCE();
      SB();
    }
    MEMBAR();
    LDS_WAIT(4, SETUP_DONE, 0);
#pragma unroll
    for (int i = 0; i < 6; ++i) {
      wop[i][0] = *reinterpret_cast<const u32x4*>(wl + (2 * i) * 256);
      wop[i][1] = *reinterpret_cast<const u32x4*>(wl + (2 * i + 1) * 256);
    }
    wv = *reinterpret_cast<const f32x4*>(lds + L_W + 16 + ((wave >> 1) & 1) * 8 + (lane >> 5) * 4);
    const f32x4 fw = *reinterpret_cast<const f32x4*>(lds + L_F);
    {
      float pl[2 + EB];
#pragma unroll
      for (int jb = 0; jb < 2 + EB; ++jb) pl[jb] = sc[8 * jb];
      MEMBAR();
      SB();
#pragma unroll
      for (int jb = 0; jb < 2 + EB; ++jb) VSTEP(buf[jb], 0, pl[jb]);
    }
    unsigned arrived = 0;
    FLUSH(1, arrived);
    f32x4 v[NSW];
    {
      const int d4 = lane < 48 ? 4 * lane : 0;
#pragma unroll
      for (int w = 0; w < NSW; ++w) v[w] = *reinterpret_cast<const f32x4*>(lds + L_RED + (NSW + w) * DH + d4);
      asm volatile("" : "+v"(arrived), "+v"(v[0]), "+v"(v[1]), "+v"(v[2]), "+v"(v[3]) :: "memory");
    }
    if (__builtin_amdgcn_readfirstlane(arrived) == NSW - 1) {
      const f32x4 o = (v[0] * fw.x + v[1] * fw.y) + (v[2] * fw.z + v[3] * fw.w);
      ST128((u32x4{vbit(o.x), vbit(o.y), vbit(o.z), vbit(o.w)}), rw, lane < 48 ? lane * 16 : OOB, WS_P + bid * 2048 + 1024);
      MEMBAR();
      ldu[L_CNT + 5] = 1u;
    }
    LDS_WAIT(5, 1, 0);
  } else {
    if (wave < 3) {
      const __amdgpu_buffer_rsrc_t rq = __builtin_amdgcn_make_buffer_rsrc((void*)wq, 0, D * H * DH * 2, RSRC3);
      unsigned xs[2][5], xp[2];
#pragma unroll
      for (int bb = 0; bb < 2; ++bb) {
#pragma unroll
        for (int i = 0; i < 5; ++i) xs[bb][i] = __builtin_amdgcn_raw_buffer_load_b32(rx, lane * 4 + i * 256 + bb * D * 2, 0, 0);
        xp[bb] = __builtin_amdgcn_raw_buffer_load_b32(rx, (c < 5 ? (8 * c + r) * 4 : 0) + bb * D * 2, qp * 160, 0);
      }
      const u32x2 ep = __builtin_amdgcn_raw_buffer_load_b64(rw, vzero, WS_EPOCH, AUX_SC1);
      SB();
      u32x4 wqr[5][2];
      {
        const unsigned vo = (unsigned)(2 * r) * (H * DH * 2) + c * 16;
        const unsigned qso = (unsigned)(80 * qp) * (H * DH * 2) + qh * (DH * 2) + wave * 128;
#pragma unroll
        for (int i = 0; i < 5; ++i)
#pragma unroll
          for (int e = 0; e < 2; ++e) { wqr[i][e] = LD128(rq, vo, qso + (16 * i + e) * (H * DH * 2), AUX_NT); SB(); }
      }
      float s0 = 0.f, s1 = 0.f, q0 = 0.f, q1 = 0.f;
#pragma unroll
      for (int i = 0; i < 5; ++i) {
        s0 = dot2(xs[0][i], ONES, s0); q0 = dot2(xs[0][i], xs[0][i], q0);
        s1 = dot2(xs[1][i], ONES, s1); q1 = dot2(xs[1][i], xs[1][i], q1);
      }
      const float st = row_sum16(fold16(fold32(s0, s1), fold32(q0, q1)));
      auto sw2 = __builtin_amdgcn_permlane16_swap(__float_as_uint(st), __float_as_uint(st), false, false);
      const float mean = __uint_as_float(sw2[0]) * (1.0f / D);
      const float var = fmaxf(fmaf(-mean, mean, __uint_as_float(sw2[1]) * (1.0f / D)), 0.f);
      const float inv = ln_den_inv(var), mb = rbf(mean);
      const float m0 = rdlane(mb, 0), m1 = rdlane(mb, 32), i0 = rdlane(inv, 0), i1 = rdlane(inv, 32);
      const unsigned xn0 = pack_bf16(rbf(bflo(xp[0]) - m0) * i0, rbf(bfhi(xp[0]) - m0) * i0);
      const unsigned xn1 = pack_bf16(rbf(bflo(xp[1]) - m1) * i1, rbf(bfhi(xp[1]) - m1) * i1);
      const unsigned long long tg = mix64(((unsigned long long)(unsigned)__builtin_amdgcn_readfirstlane(ep.y) << 32) | (unsigned)__builtin_amdgcn_readfirstlane(ep.x));
      const unsigned thi = (unsigned)(tg >> 32), t8 = (unsigned)tg & 0xff000000u;
      float a0[8], a1q[8];
#pragma unroll
      for (int k = 0; k < 8; ++k) { a0[k] = 0.f; a1q[k] = 0.f; }
#define QGROUP(I) { \
        unsigned b0_ = __builtin_amdgcn_update_dpp(0u, xn0, 0x150 + I, 0xf, 0x3, false); b0_ = __builtin_amdgcn_update_dpp(b0_, xn0, 0x158 + I, 0xf, 0xc, false); \
        unsigned b1_ = __builtin_amdgcn_update_dpp(0u, xn1, 0x150 + I, 0xf, 0x3, false); b1_ = __builtin_amdgcn_update_dpp(b1_, xn1, 0x158 + I, 0xf, 0xc, false); \
        _Pragma("unroll") for (int j = 0; j < 4; ++j) { \
          const unsigned plo = __builtin_amdgcn_perm(wqr[I][1][j], wqr[I][0][j], 0x05040100u), phi = __builtin_amdgcn_perm(wqr[I][1][j], wqr[I][0][j], 0x07060302u); \
          a0[2 * j] = dot2(plo, b0_, a0[2 * j]); a0[2 * j + 1] = dot2(phi, b0_, a0[2 * j + 1]); \
          a1q[2 * j] = dot2(plo, b1_, a1q[2 * j]); a1q[2 * j + 1] = dot2(phi, b1_, a1q[2 * j + 1]); } }
      QGROUP(0) QGROUP(1) QGROUP(2) QGROUP(3) QGROUP(4)
      float z[8];
#pragma unroll
      for (int k = 0; k < 8; ++k) z[k] = fold32(a0[k], a1q[k]);
      float y[4];
#pragma unroll
      for (int k = 0; k < 4; ++k) y[k] = fold16(z[k], z[k + 4]);
      const float u0 = fold8(y[0], y[2], bit3), u1 = fold8(y[1], y[3], bit3);
      const unsigned pidx = 32 * wave + 4 * c + 2 * ((lane >> 4) & 1) + (bit3 ? 1 : 0);
      ST128(pack2(u0, u1, t8, thi), rw, ((lane >> 5) * 8 * 96 + pidx) * 16, WS_QP + (qh * 16 + qp) * (96 * 16));
      asm volatile("s_waitcnt vmcnt(0)" ::: "memory");
      {
        const unsigned qo = ((lane >> 5) * 8 * 96 + 32 * wave + (lane & 31)) * 16;
        const unsigned qb = WS_QP + h * 16 * (96 * 16);
        u32x4 gq[8];
        for (unsigned spins = 0;; ++spins) {
          MEMBAR();
#pragma unroll
          for (int k = 0; k < 8; ++k) gq[k] = LD128(rw, qo, qb + k * (96 * 16), AUX_SC1);
          unsigned bad = 0;
#pragma unroll
          for (int k = 0; k < 8; ++k) bad |= tag_bad(gq[k], t8, thi);
          if (__all(bad == 0u) || spins > SPIN_CAP) break;
        }
        const f32x2 qq = ((g01(gq[0]) + g01(gq[2])) + (g01(gq[4]) + g01(gq[6]))) + ((g01(gq[1]) + g01(gq[3])) + (g01(gq[5]) + g01(gq[7])));
        ldu[L_Q + (lane >> 5) * 96 + 32 * wave + (lane & 31)] = pack_bf16(qq.x, qq.y);
      }
      if (tid == 0) *reinterpret_cast<u32x2*>(ldu + L_TAG) = u32x2{(unsigned)tg, thi};
    }
    const unsigned yoff = ((lane >> 5) * 8 * 320 + (lane & 31)) * 16;
    const unsigned ybo = WS_Y + (team * 320 + pc * 32) * 16 + b * 8;
    if (wave == 3) {
      if (lane < 8) ldu[L_CNT + lane] = 0u;
      const u32x2 ep = LD64(rw, vzero, WS_EPOCH, AUX_SC1);
      ST128((u32x4{0u, 0u, 0u, 0u}), rw, lane * 16, WS_P + bid * 2048);
      ST128((u32x4{0u, 0u, 0u, 0u}), rw, lane * 16, WS_P + bid * 2048 + 1024);
      ST64((u32x2{0u, 0u}), rw, proj ? yoff : OOB, ybo);
      asm volatile("s_waitcnt vmcnt(0)" ::: "memory");
      const unsigned long long tg = mix64(((unsigned long long)(unsigned)__builtin_amdgcn_readfirstlane(ep.y) << 32) | (unsigned)__builtin_amdgcn_readfirstlane(ep.x));
      ST64((u32x2{(unsigned)tg, (unsigned)(tg >> 32)}), rw, lane == 0 ? 0u : OOB, WS_RDY + (team * 32 + jj) * 8);
    }
    __syncthreads();
    LOAD_SMALL();
    const __amdgpu_buffer_rsrc_t ro = __builtin_amdgcn_make_buffer_rsrc((void*)wo, 0, D * H * DH * 2, RSRC3);
    const unsigned vo = (unsigned)(2 * r) * (D * 2) + c * 16;
    const unsigned wso = !proj ? OOB : (unsigned)(768 * team + 384 * b + 96 * wave) * (D * 2) + pc * 128;
#pragma unroll
    for (int i = 0; i < 6; ++i) {
      {
        const unsigned slot = (unsigned)(__builtin_amdgcn_s_memrealtime() >> WOSLOT);
        for (int spins = 0; spins < 1024 && (unsigned)(__builtin_amdgcn_s_memrealtime() >> WOSLOT) == slot; ++spins) __builtin_amdgcn_s_sleep(4);
      }
      const u32x4 e0 = LD128(ro, vo, wso + 16 * i * (D * 2), AUX_NT);
      const u32x4 e1 = LD128(ro, vo + D * 2, wso + 16 * i * (D * 2), AUX_NT);
      u32x4 p0, p1;
      p0.x = __builtin_amdgcn_perm(e1.x, e0.x, 0x05040100u); p0.y = __builtin_amdgcn_perm(e1.x, e0.x, 0x07060302u);
      p0.z = __builtin_amdgcn_perm(e1.y, e0.y, 0x05040100u); p0.w = __builtin_amdgcn_perm(e1.y, e0.y, 0x07060302u);
      p1.x = __builtin_amdgcn_perm(e1.z, e0.z, 0x05040100u); p1.y = __builtin_amdgcn_perm(e1.z, e0.z, 0x07060302u);
      p1.z = __builtin_amdgcn_perm(e1.w, e0.w, 0x05040100u); p1.w = __builtin_amdgcn_perm(e1.w, e0.w, 0x07060302u);
      wop[i][0] = p0; wop[i][1] = p1;
      *reinterpret_cast<u32x4*>(wl + (2 * i) * 256) = p0;
      *reinterpret_cast<u32x4*>(wl + (2 * i + 1) * 256) = p1;
      MEMBAR();
      SB();
    }
    if (lane == 0) LDS_ARRIVE(4);
    if (wave == 0) {
      const u32x2 tg = *reinterpret_cast<const u32x2*>(ldu + L_TAG);
      for (unsigned spins = 0; spins < SPIN_CAP; ++spins) {
        MEMBAR();
        const u32x4 r0 = LD128(rw, lane * 16, WS_RDY, AUX_SC1), r1 = LD128(rw, lane * 16 + 1024, WS_RDY, AUX_SC1);
        const unsigned bad = (r0.x ^ tg.x) | (r0.y ^ tg.y) | (r0.z ^ tg.x) | (r0.w ^ tg.y) | (r1.x ^ tg.x) | (r1.y ^ tg.y) | (r1.z ^ tg.x) | (r1.w ^ tg.y);
        if (__all(bad == 0u)) break;
        __builtin_amdgcn_s_sleep(32);
      }
      if (lane == 0) { LDS_ARRIVE(6); LDS_ARRIVE(4); }
    }
    LDS_WAIT(0, NSW, 16);
    if (wave == 1) {
      const int d4 = lane < 48 ? 4 * lane : 0;
      const f32x4 ma = *reinterpret_cast<const f32x4*>(lds + L_ML + (lane >> 5) * 8), mc = *reinterpret_cast<const f32x4*>(lds + L_ML + (lane >> 5) * 8 + 4);
      f32x4 v[NSW];
#pragma unroll
      for (int w = 0; w < NSW; ++w) v[w] = *reinterpret_cast<const f32x4*>(lds + L_RED + w * DH + d4);
      const float M = __uint_as_float(vbit(fmaxf(fmaxf(ma.x, ma.z), fmaxf(mc.x, mc.z))));
      const f32x4 f = {__builtin_amdgcn_exp2f(ma.x - M), __builtin_amdgcn_exp2f(ma.z - M), __builtin_amdgcn_exp2f(mc.x - M), __builtin_amdgcn_exp2f(mc.z - M)};
      const float Ls = (ma.y * f.x + ma.w * f.y) + (mc.y * f.z + mc.w * f.w);
      if (lane == 32) *reinterpret_cast<f32x4*>(lds + L_F) = f;
      f32x4 o = (v[0] * rdlane(f.x, 0) + v[1] * rdlane(f.y, 0)) + (v[2] * rdlane(f.z, 0) + v[3] * rdlane(f.w, 0));
      const float M0 = rdlane(M, 0), L0 = rdlane(Ls, 0);
      if (lane >= 48) o = f32x4{M0, L0, M, Ls};
      ST128((u32x4{vbit(o.x), vbit(o.y), vbit(o.z), vbit(o.w)}), rw, lane <= 48 ? lane * 16 : OOB, WS_P + bid * 2048);
      if (lane == 0) LDS_ARRIVE(4);
    }
    __builtin_amdgcn_s_sleep(127);
    LDS_WAIT(6, 1, 16);
    if (proj) {
      const unsigned mbase = WS_P + (unsigned)(team + 64 * (2 * b + (wave >> 1))) * 2048 + 48 * 16;
      u32x2 gm;
      for (unsigned spins = 0; spins < SPIN_CAP; ++spins) {
        MEMBAR();
        gm = LD64(rw, (lane & 7) * (8 * 2048) + (lane & 8), mbase, AUX_SC1);
        if (__all((gm.x & gm.y & 1u) != 0u)) break;
        __builtin_amdgcn_s_sleep(32);
      }
      const float ms = __uint_as_float(gm.x);
      float M = fmaxf(ms, dpp<XOR1>(ms)); M = fmaxf(M, dpp<XOR2>(M)); M = fmaxf(M, dpp<HMIR>(M));
      const float f = __builtin_amdgcn_exp2f(ms - M);
      float Ls = f * __uint_as_float(gm.y);
      Ls += dpp<XOR1>(Ls); Ls += dpp<XOR2>(Ls); Ls += dpp<HMIR>(Ls);
      if (lane < 16) lds[L_W + (lane >> 3) * 16 + (wave >> 1) * 8 + (lane & 1) * 4 + ((lane >> 1) & 3)] = f * __builtin_amdgcn_rcpf(Ls);
      MEMBAR();
      wv = *reinterpret_cast<const f32x4*>(lds + L_W + (wave >> 1) * 8 + (lane >> 5) * 4);
    }
    if (lane == 0) LDS_ARRIVE(4);
  }
  const u32x2 tg2 = *reinterpret_cast<const u32x2*>(ldu + L_TAG);
  const unsigned tlo = __builtin_amdgcn_readfirstlane(tg2.x), thi = __builtin_amdgcn_readfirstlane(tg2.y);

  if (proj) {
    const unsigned pbase = WS_P + (unsigned)(team + 64 * (2 * b + ((wave >> 1) & 1))) * 2048 + (wave >> 2) * 1024 + (wave & 1) * 384;
    const int j = lane & 31;
    const unsigned po = (lane >> 5) * (8 * 2048) + (j < 24 ? j : 0) * 16;
    u32x4 go[4];
    unsigned pend = 0xfu, spins = 0;
    do {
      MEMBAR();
#pragma unroll
      for (int k = 0; k < 4; ++k)
        if (pend & (1u << k)) go[k] = LD128(rw, po, pbase + k * (16 * 2048), AUX_SC1);
#pragma unroll
      for (int k = 0; k < 4; ++k)
        if (pend & (1u << k)) { if (__all((go[k].x & go[k].y & go[k].z & go[k].w & 1u) != 0u)) pend &= ~(1u << k); }
      if (wave < NH && pend != 0u) __builtin_amdgcn_s_sleep(32);
    } while (pend != 0u && ++spins < SPIN_CAP);
    const f32x4 o = (__builtin_bit_cast(f32x4, go[0]) * wv.x + __builtin_bit_cast(f32x4, go[1]) * wv.y) +
                    (__builtin_bit_cast(f32x4, go[2]) * wv.z + __builtin_bit_cast(f32x4, go[3]) * wv.w);
    const unsigned pr = pack_bf16(fold32(o.x, o.z), fold32(o.y, o.w));
    if (j < 24) ldu[L_O + 48 * wave + 2 * j + (lane >> 5)] = pr;
    MEMBAR();
  }
  if (proj) {
    unsigned op[6];
#pragma unroll
    for (int i = 0; i < 6; ++i) op[i] = ldu[L_O + 48 * wave + 8 * i + r];
    float y[8];
#pragma unroll
    for (int k = 0; k < 8; ++k) y[k] = 0.f;
#pragma unroll
    for (int i = 0; i < 6; ++i) {
#pragma unroll
      for (int k = 0; k < 4; ++k) { y[k] = dot2(wop[i][0][k], op[i], y[k]); y[k + 4] = dot2(wop[i][1][k], op[i], y[k + 4]); }
    }
    const float z0 = fold32(y[0], y[4]), z1 = fold32(y[1], y[5]), z2 = fold32(y[2], y[6]), z3 = fold32(y[3], y[7]);
    const float u = fold8(fold16(z0, z2), fold16(z1, z3), bit3);
    lds[L_YR + wave * 64 + fidx] = u;
    const int bb = wave >> 2;
    MEMBAR();
    unsigned arrived = LDS_ARRIVE_FAST(2 + bb);
    MEMBAR();
    f32x2 v[4];
#pragma unroll
    for (int w = 0; w < 4; ++w) v[w] = *reinterpret_cast<const f32x2*>(lds + L_YR + (4 * bb + w) * 64 + 2 * (lane & 31));
    asm volatile("" : "+v"(arrived), "+v"(v[0]), "+v"(v[1]), "+v"(v[2]), "+v"(v[3]) :: "memory");
    if (__builtin_amdgcn_readfirstlane(arrived) == 3) {
      const f32x2 ys = (v[0] + v[1]) + (v[2] + v[3]);
      ST64((u32x2{vbit(ys.x), vbit(ys.y)}), rw, lane < 32 ? lane * 16 : OOB, WS_Y + ((bb * 8 + team) * 320 + pc * 32) * 16 + b * 8);
    }
  }
  __syncthreads();
  CONV_SMALL();
  if (wave >= 3) {
    u32x4 gy[8];
    const unsigned yo = (tid - 192) * 16, ybase = WS_Y + b * (8 * 320 * 16);
    unsigned pend = 0xffu, spins = 0;
    do {
      MEMBAR();
#pragma unroll
      for (int s = 0; s < 8; ++s)
        if (pend & (1u << s)) gy[s] = LD128(rw, yo, ybase + s * (320 * 16), AUX_SC1);
#pragma unroll
      for (int s = 0; s < 8; ++s)
        if (pend & (1u << s)) { if (__all((gy[s].x & gy[s].y & gy[s].z & gy[s].w & 1u) != 0u)) pend &= ~(1u << s); }
    } while (pend != 0u && ++spins < SPIN_CAP);
    f32x2 ap[8];
#pragma unroll
    for (int s = 0; s < 8; ++s) ap[s] = asf2(gy[s].x, gy[s].y) + asf2(gy[s].z, gy[s].w);
    const f32x2 a = ((ap[0] + ap[1]) + (ap[2] + ap[3])) + ((ap[4] + ap[5]) + (ap[6] + ap[7]));
    const unsigned hp = pack_bf16(bflo(xres) + rbf(a.x), bfhi(xres) + rbf(a.y));
    ldu[L_H + tid - 192] = hp;
    float st = fold32(dot2(hp, ONES, 0.f), dot2(hp, hp, 0.f));
    st = row_sum16(fold16(st, st));
    if ((lane & 31) == 0) lds[L_ST + (wave - 3) * 2 + (lane >> 5)] = st;
  }
  __syncthreads();
  const int d0 = 5 * (team * 16 + cb);
  float he;
  {
    const int de = d0 + (lane < 5 ? lane : 0);
    const unsigned hw = ldu[L_H + (de >> 1)];
    f32x2 sv[5];
#pragma unroll
    for (int w = 0; w < 5; ++w) sv[w] = *reinterpret_cast<const f32x2*>(lds + L_ST + 2 * w);
    const f32x2 st = (((sv[0] + sv[1]) + (sv[2] + sv[3])) + sv[4]) * splat(1.0f / D);
    he = (de & 1) ? bfhi(hw) : bflo(hw);
    const float mean1 = st.x;
    const float var1 = fmaxf(fmaf(-mean1, mean1, st.y), 0.f);
    const float i1 = ln_den_inv(var1), m1 = rbf(mean1);
    const float m2 = rbf((mean1 - m1) * i1), i2 = ln_den_inv(var1 * i1 * i1);
    const float tl = rbf(rbf(rbf(rbf(he - m1) * i1) - m2) * i2);
    constexpr float G1 = -2.0f * 0.7978845608028654f * LOG2E, G3 = G1 * 0.044715f;
    float te[5];
#pragma unroll
    for (int e = 0; e < 5; ++e) te[e] = rdlane(tl, e);
    f32x2 u[8], z[8];
#pragma unroll
    for (int e = 0; e < 5; ++e) u[e] = fma2(splat(te[e]), fw1, fb1);
    u[5] = fma2(f32x2{te[0], te[1]}, splat(gw1), splat(gb1));
    u[6] = fma2(f32x2{te[2], te[3]}, splat(gw1), splat(gb1));
    u[7] = fma2(splat(te[4]), splat(gw1), splat(gb1));
    SB();
#pragma unroll
    for (int k = 0; k < 8; ++k) z[k] = u[k] * u[k];
    SB();
#pragma unroll
    for (int k = 0; k < 8; ++k) z[k] = fma2(z[k], splat(G3), splat(G1));
    SB();
#pragma unroll
    for (int k = 0; k < 8; ++k) z[k] = z[k] * u[k];
    SB();
#pragma unroll
    for (int k = 0; k < 8; ++k) { z[k].x = __builtin_amdgcn_exp2f(z[k].x); if (k < 7) z[k].y = __builtin_amdgcn_exp2f(z[k].y); }
#pragma unroll
    for (int k = 0; k < 8; ++k) u[k] = u[k] * (k < 5 ? fw2 : splat(gw2));
    SB();
#pragma unroll
    for (int k = 0; k < 8; ++k) z[k] = z[k] + splat(1.0f);
    SB();
#pragma unroll
    for (int k = 0; k < 8; ++k) { z[k].x = __builtin_amdgcn_rcpf(z[k].x); if (k < 7) z[k].y = __builtin_amdgcn_rcpf(z[k].y); }
    SB();
#pragma unroll
    for (int k = 0; k < 8; ++k) u[k] = u[k] * z[k];
    const float ya0 = (u[0].x + u[0].y) + u[5].x, ya1 = (u[1].x + u[1].y) + u[5].y, ya2 = (u[2].x + u[2].y) + u[6].x,
                ya3 = (u[3].x + u[3].y) + u[6].y, ya4 = (u[4].x + u[4].y) + u[7].x;
    const float f4 = fold32(ya4, ya4);
    float r0 = fold8(fold16(fold32(ya0, ya1), fold32(ya2, ya3)), fold16(f4, f4), bit3);
    r0 += dpp<XOR1>(r0); r0 += dpp<XOR2>(r0); r0 += dpp<HMIR>(r0);
    const int el = bit3 ? 4 : (lane >> 5) + 2 * ((lane >> 4) & 1);
    if ((lane & 15) == 0 || lane == 8) lds[L_MR + el * 8 + wave] = r0;
  }
  MEMBAR();
  unsigned arrived5 = LDS_ARRIVE_FAST(7);
  MEMBAR();
  f32x4 pa = *reinterpret_cast<const f32x4*>(lds + L_MR + (lane & 7) * 8), pb = *reinterpret_cast<const f32x4*>(lds + L_MR + (lane & 7) * 8 + 4);
  asm volatile("" : "+v"(arrived5), "+v"(pa), "+v"(pb) :: "memory");
  if (__builtin_amdgcn_readfirstlane(arrived5) == NWAVE - 1 && lane < 5) {
    const f32x4 ps = pa + pb;
    const float yv = (ps.x + ps.y) + (ps.z + ps.w);
    const unsigned o = pack_bf16(he + rbf(rbf(yv) + fb2), 0.f);
    __builtin_amdgcn_raw_buffer_store_b16((unsigned short)o, __builtin_amdgcn_make_buffer_rsrc((void*)out, 0, 2 * D * 2 + 4, RSRC3),
                                          lane * 2, (b * D + d0) * 2, 0);
  }
  if (tid == 0 && bid == 0)
    __builtin_amdgcn_raw_buffer_store_b64(u32x2{tlo, thi}, rw, 0, WS_EPOCH, AUX_SC1);
}

}

extern "C" void kernel_launch(void* const* d_in, const int* in_sizes, int n_in, void* d_out, int out_size, void* d_ws,
                              size_t ws_size, hipStream_t stream) {
  (void)in_sizes; (void)n_in; (void)out_size; (void)ws_size;
  fused_kernel<<<dim3(NWG), dim3(NTHR), 0, stream>>>(
      (const char*)d_in[0], (const char*)d_in[1], (const char*)d_in[2], (const char*)d_in[3], (const char*)d_in[4],
      (const char*)d_in[6], (const char*)d_in[7], (const char*)d_in[8], (const char*)d_in[9], (char*)d_out, (char*)d_ws);
}
```

```cpp
#include <hip/hip_runtime.h>
#include <hip/hip_bf16.h>
#include <stdint.h>

namespace {

typedef unsigned u32x4 __attribute__((ext_vector_type(4)));
typedef unsigned u32x2 __attribute__((ext_vector_type(2)));
typedef float f32x2 __attribute__((ext_vector_type(2)));
typedef float f32x4 __attribute__((ext_vector_type(4)));
typedef __bf16 bf16x2 __attribute__((ext_vector_type(2)));

constexpr int D = 640, H = 32, SK = 8192, DH = 192, FF = 1536;
constexpr int NWG = 256, NTHR = 512, NWAVE = 8;
constexpr unsigned TILE_BYTES = 8 * DH * 2;
constexpr unsigned HEAD_BYTES = SK * DH * 2;
constexpr int RA = 8;
constexpr int RPRE = 8;
constexpr int EB = 4;
constexpr int TKI = 48;
constexpr int NUNIT = 20;
constexpr int GS = 8;
constexpr int NSW = 4, NH = NWAVE - NSW;
constexpr int WSTEPS = SK / 8 / (GS * NSW);
constexpr int STEPS = 2 * WSTEPS;
constexpr unsigned STRIDE = GS * NSW * TILE_BYTES;
constexpr int AUX_NT = 2, AUX_SC1 = 16;
constexpr unsigned RSRC3 = 0x00020000u;
constexpr unsigned OOB = 0x80000000u;

constexpr unsigned WS_RDY = 4096;
constexpr unsigned WS_TK = WS_RDY + 2560;
constexpr unsigned WS_P = 8192;
constexpr unsigned WS_Y = WS_P + NWG * 2048;
constexpr unsigned WS_QP = WS_Y + 2 * 8 * 320 * 16 + 128;
constexpr unsigned WS_END = WS_QP + 32 * 2 * 8 * 96 * 16 + 128;
constexpr unsigned WS_E0 = WS_RDY - 128, WS_E1 = WS_RDY + NWG * 8, WS_E2 = WS_QP - 128, WS_E3 = WS_END - 128;

constexpr int L_RED = 0;
constexpr int L_ML = L_RED + 2 * NSW * DH;
constexpr int L_Q = L_ML + 16;
constexpr int L_TAG = L_Q + 192;
constexpr int L_CNT = L_TAG + 4;
constexpr int L_W = L_CNT + 16;
constexpr int L_F = L_W + 32;
constexpr int L_O = L_F + 4;
constexpr int L_YR = L_O + 384;
constexpr int L_H = L_YR + 512;
constexpr int L_ST = L_H + 320;
constexpr int L_MR = L_ST + 16;
constexpr int L_DUM = L_MR + 64;
constexpr int L_SC = L_DUM + 64;
constexpr int L_WO = L_SC + NSW * 512;
constexpr int L_TOTAL = 24 * 1024;
static_assert(L_WO + 24 * 2 * 64 * 4 <= L_TOTAL, "lds");
static_assert(L_WO % 4 == 0 && L_Q % 4 == 0 && L_ST % 4 == 0 && L_MR % 4 == 0, "alignment");

__device__ __forceinline__ float dot2(unsigned a, unsigned b, float c) {
  return __builtin_amdgcn_fdot2_f32_bf16(__builtin_bit_cast(bf16x2, a), __builtin_bit_cast(bf16x2, b), c, false);
}
template <int CTRL> __device__ __forceinline__ float dpp(float x) {
  return __builtin_bit_cast(float, __builtin_amdgcn_mov_dpp(__builtin_bit_cast(int, x), CTRL, 0xf, 0xf, true));
}
constexpr int XOR1 = 0xB1, XOR2 = 0x4E, HMIR = 0x141, ROR8 = 0x128;
constexpr unsigned ONES = 0x3f803f80u;
__device__ __forceinline__ float bflo(unsigned u) { return __builtin_bit_cast(float, u << 16); }
__device__ __forceinline__ float bfhi(unsigned u) { return __builtin_bit_cast(float, u & 0xffff0000u); }
__device__ __forceinline__ f32x2 bf2(unsigned u) { return f32x2{bflo(u), bfhi(u)}; }
__device__ __forceinline__ unsigned pack_bf16(float lo, float hi) {
  bf16x2 v; v[0] = (__bf16)lo; v[1] = (__bf16)hi;
  return __builtin_bit_cast(unsigned, v);
}
__device__ __forceinline__ float rbf(float x) {
  typedef float f32x2_ __attribute__((ext_vector_type(2)));
  return __builtin_bit_cast(float, __builtin_convertvector(f32x2_{0.f, x}, bf16x2));
}
__device__ __forceinline__ float fold32(float a, float b) {
  auto r = __builtin_amdgcn_permlane32_swap(__float_as_uint(a), __float_as_uint(b), false, false);
  return __uint_as_float(r[0]) + __uint_as_float(r[1]);
}
__device__ __forceinline__ float fold16(float a, float b) {
  auto r = __builtin_amdgcn_permlane16_swap(__float_as_uint(a), __float_as_uint(b), false, false);
  return __uint_as_float(r[0]) + __uint_as_float(r[1]);
}
__device__ __forceinline__ float fold8(float a, float b, bool bit3) {
  const float keep = bit3 ? b : a, send = bit3 ? a : b;
  return keep + dpp<ROR8>(send);
}
__device__ __forceinline__ float max3216(float x) {
  auto s = __builtin_amdgcn_permlane16_swap(__float_as_uint(x), __float_as_uint(x), false, false);
  x = fmaxf(__uint_as_float(s[0]), __uint_as_float(s[1]));
  auto t = __builtin_amdgcn_permlane32_swap(__float_as_uint(x), __float_as_uint(x), false, false);
  return fmaxf(__uint_as_float(t[0]), __uint_as_float(t[1]));
}
__device__ __forceinline__ float row_sum16(float x) {
  x += dpp<XOR1>(x); x += dpp<XOR2>(x); x += dpp<HMIR>(x); x += dpp<ROR8>(x);
  return x;
}
__device__ __forceinline__ unsigned long long mix64(unsigned long long z) {
  z += 0x9E3779B97F4A7C15ull;
  z = (z ^ (z >> 30)) * 0xBF58476D1CE4E5B9ull;
  z = (z ^ (z >> 27)) * 0x94D049BB133111EBull;
  return z ^ (z >> 31);
}
__device__ __forceinline__ unsigned epoch_off(int lane) {
  return (lane & 2) ? ((lane & 1) ? WS_E3 : WS_E2) : ((lane & 1) ? WS_E1 : WS_E0);
}
__device__ __forceinline__ unsigned long long launch_tag(u32x2 e) {
  unsigned long long z = 0;
#pragma unroll
  for (int k = 0; k < 4; ++k)
    z = z * 0x9E3779B97F4A7C15ull + (((unsigned long long)(unsigned)__builtin_amdgcn_readlane(e.y, k) << 32) | (unsigned)__builtin_amdgcn_readlane(e.x, k));
  return mix64(z);
}
__device__ __forceinline__ u32x4 pack2(float a, float b, unsigned t8, unsigned thi) {
  u32x4 r; r.x = (__float_as_uint(a) >> 8) | t8; r.y = thi; r.z = (__float_as_uint(b) >> 8) | t8; r.w = thi; return r;
}
__device__ __forceinline__ unsigned tag_bad(u32x4 g, unsigned t8, unsigned thi) {
  return (((g.x ^ t8) | (g.z ^ t8)) >> 24) | (g.y ^ thi) | (g.w ^ thi);
}
__device__ __forceinline__ float g0(u32x4 g) { return __uint_as_float(g.x << 8); }
__device__ __forceinline__ float g1(u32x4 g) { return __uint_as_float(g.z << 8); }
__device__ __forceinline__ f32x2 g01(u32x4 g) { return f32x2{g0(g), g1(g)}; }
__device__ __forceinline__ unsigned vbit(float x) { return __float_as_uint(x) | 1u; }
__device__ __forceinline__ f32x2 asf2(unsigned a, unsigned b) { return f32x2{__uint_as_float(a), __uint_as_float(b)}; }
__device__ __forceinline__ float ln_den_inv(float var) {
  return __builtin_amdgcn_rcpf(rbf(__builtin_amdgcn_sqrtf(rbf(rbf(var) + 1e-5f))));
}
__device__ __forceinline__ float rdlane(float x, int l) { return __uint_as_float(__builtin_amdgcn_readlane(__float_as_uint(x), l)); }
__device__ __forceinline__ f32x2 splat(float x) { return f32x2{x, x}; }
__device__ __forceinline__ f32x2 fma2(f32x2 a, f32x2 b, f32x2 c) { return __builtin_elementwise_fma(a, b, c); }

#define SB() __builtin_amdgcn_sched_barrier(0)
#define MEMBAR() asm volatile("" ::: "memory")
#define LD128(rs, vo, so, aux) __builtin_amdgcn_raw_buffer_load_b128((rs), (vo), (so), (aux))
#define ST128(v, rs, vo, so) __builtin_amdgcn_raw_buffer_store_b128((v), (rs), (vo) + (so), 0, AUX_SC1)
#define LD64(rs, vo, so, aux) __builtin_amdgcn_raw_buffer_load_b64((rs), (vo), (so), (aux))
#define ST64(v, rs, vo, so) __builtin_amdgcn_raw_buffer_store_b64((v), (rs), (vo), (so), AUX_SC1)
#define WAIT_VM3() __builtin_amdgcn_s_waitcnt(0x0F73)

constexpr unsigned SPIN_CAP = 1u << 17;

__global__ void __launch_bounds__(NTHR, 2)
fused_kernel(const char* x, const char* kc, const char* vc, const char* wq, const char* wo, const char* w1,
             const char* b1, const char* w2, const char* b2, char* out, char* ws) {
  __shared__ __attribute__((aligned(16))) float lds[L_TOTAL];
  unsigned* const ldu = reinterpret_cast<unsigned*>(lds);
  const int tid = threadIdx.x, lane = tid & 63, wave = __builtin_amdgcn_readfirstlane(tid >> 6);
  const int bid = blockIdx.x, team = bid & 7, jj = bid >> 3, hl = jj >> 3, b = (jj >> 2) & 1, sp = jj & 3;
  const int h = team * 4 + hl, cb = hl * 4 + sp, pq = jj & 7;
  const int pc = sp * 4 + hl;
  const bool proj = pc < 10;
  const int qh = jj, qp = team;
  const int r = lane >> 3, c = lane & 7;
  const bool bit3 = (lane & 8) != 0;
  const int fidx = 8 * c + 4 * (lane >> 5) + 2 * ((lane >> 4) & 1) + (bit3 ? 1 : 0);
  asm volatile("" : "+s"(x), "+s"(kc), "+s"(vc), "+s"(wq), "+s"(wo), "+s"(w1), "+s"(b1), "+s"(w2), "+s"(b2), "+s"(out), "+s"(ws));
  const __amdgpu_buffer_rsrc_t rw = __builtin_amdgcn_make_buffer_rsrc((void*)ws, 0, WS_END, RSRC3);
  const __amdgpu_buffer_rsrc_t rx = __builtin_amdgcn_make_buffer_rsrc((void*)x, 0, 2 * D * 2, RSRC3);
  constexpr float SCALE = 0.07216878364870322f, LOG2E = 1.4426950408889634f;
  unsigned vzero;
  asm volatile("v_mov_b32 %0, 0" : "=v"(vzero));

  f32x2 fw1, fb1, fw2; float gw1, gb1, gw2, fb2;
  unsigned xres;
  unsigned short a1[3], a2[3], a3[3], a4;
#define LOAD_SMALL() { \
    const __amdgpu_buffer_rsrc_t r1 = __builtin_amdgcn_make_buffer_rsrc((void*)w1, 0, FF * 2 + 4, RSRC3); \
    const __amdgpu_buffer_rsrc_t r2 = __builtin_amdgcn_make_buffer_rsrc((void*)b1, 0, FF * 2 + 4, RSRC3); \
    const __amdgpu_buffer_rsrc_t r3 = __builtin_amdgcn_make_buffer_rsrc((void*)w2, 0, FF * 2 + 4, RSRC3); \
    _Pragma("unroll") for (int k = 0; k < 3; ++k) { \
      a1[k] = __builtin_amdgcn_raw_buffer_load_b16(r1, tid * 2 + 1024 * k, 0, 0); \
      a2[k] = __builtin_amdgcn_raw_buffer_load_b16(r2, tid * 2 + 1024 * k, 0, 0); \
      a3[k] = __builtin_amdgcn_raw_buffer_load_b16(r3, tid * 2 + 1024 * k, 0, 0); \
    } \
    a4 = __builtin_amdgcn_raw_buffer_load_b16(__builtin_amdgcn_make_buffer_rsrc((void*)b2, 0, 4, RSRC3), vzero, 0, 0); \
    xres = __builtin_amdgcn_raw_buffer_load_b32(rx, tid >= 192 ? (tid - 192) * 4 : OOB, b * D * 2, 0); \
    MEMBAR(); }
#define CONV_SMALL() { \
    fw1 = f32x2{bflo(a1[0]), bflo(a1[1])}; fb1 = f32x2{bflo(a2[0]), bflo(a2[1])}; fw2 = f32x2{bflo(a3[0]), bflo(a3[1])}; \
    gw1 = bflo(a1[2]); gb1 = bflo(a2[2]); gw2 = bflo(a3[2]); fb2 = bflo(a4); \
    asm volatile("" : "+v"(fw1), "+v"(fb1), "+v"(fw2), "+v"(gw1), "+v"(gb1), "+v"(gw2), "+v"(fb2), "+v"(xres) :: "memory"); }

#define LDS_WAIT(IDX, VAL, SLEEP) { unsigned spins_ = 0; \
    _Pragma("nounroll") while (__hip_atomic_load(ldu + L_CNT + (IDX), __ATOMIC_ACQUIRE, __HIP_MEMORY_SCOPE_WORKGROUP) != (unsigned)(VAL) && ++spins_ < (1u << 24) / (1u + (SLEEP))) \
      { if (SLEEP) __builtin_amdgcn_s_sleep(SLEEP); } }
#define LDS_ARRIVE_FAST(IDX) __hip_atomic_fetch_add(ldu + (lane == 0 ? L_CNT + (IDX) : L_DUM + lane), 1u, __ATOMIC_RELAXED, __HIP_MEMORY_SCOPE_WORKGROUP)
#define GET_UNIT(SLEEP) { unsigned spins_ = 0, u_; \
    _Pragma("nounroll") while ((u_ = __hip_atomic_load(ldu + L_CNT + 10, __ATOMIC_ACQUIRE, __HIP_MEMORY_SCOPE_WORKGROUP)) == 0u && ++spins_ < (1u << 24) / (1u + (SLEEP))) \
      { if (SLEEP) __builtin_amdgcn_s_sleep(SLEEP); } \
    unit = __builtin_amdgcn_readfirstlane(u_) - 1u; }
#define LDS_ARRIVE(IDX) __hip_atomic_fetch_add(ldu + L_CNT + (IDX), 1u, __ATOMIC_ACQ_REL, __HIP_MEMORY_SCOPE_WORKGROUP)
  unsigned unit;
  u32x4 wop[6][2];
  f32x4 wv;
  unsigned* const wl = ldu + L_WO + ((wave & 3) * 12 * 64 + lane) * 4;
  const int sw = wave - NH;
  const size_t kvbase = (size_t)h * HEAD_BYTES;
  constexpr unsigned ROW1 = H * HEAD_BYTES;
  const __amdgpu_buffer_rsrc_t rk = __builtin_amdgcn_make_buffer_rsrc((void*)(kc + kvbase), 0, ROW1 + HEAD_BYTES, RSRC3);
  const unsigned kvo = r * (DH * 2) + c * 16;
  u32x4 buf[RPRE][3];
  unsigned so = (pq * NSW + sw) * TILE_BYTES + (h & 1) * RA * STRIDE;
  if (wave >= NH) {
    const __amdgpu_buffer_rsrc_t rv = __builtin_amdgcn_make_buffer_rsrc((void*)(vc + kvbase), 0, ROW1 + HEAD_BYTES, RSRC3);
    unsigned wb = 0;
    static_assert(RA + RPRE + 2 < WSTEPS && RPRE % 2 == 0 && RPRE >= 2, "no wrap before the main loop");
#pragma unroll
    for (int j = 0; j < RPRE; ++j) {
      if (j >= 2) { WAIT_VM3(); SB(); }
#pragma unroll
      for (int t = 0; t < 3; ++t) { buf[j][t] = LD128(rk, kvo + t * 128, so, AUX_NT); SB(); }
      so += STRIDE;
    }
    LOAD_SMALL();
    __syncthreads();
    u32x4 q[3];
#pragma unroll
    for (int t = 0; t < 3; ++t) q[t] = *reinterpret_cast<const u32x4*>(ldu + L_Q + (8 * t + c) * 4);
    float* sc = lds + L_SC + sw * 512 + r;

#define PIN12(A) asm volatile("" : "+v"(A[0]), "+v"(A[1]), "+v"(A[2]), "+v"(A[3]), "+v"(A[4]), "+v"(A[5]), "+v"(A[6]), "+v"(A[7]), \
    "+v"(A[8]), "+v"(A[9]), "+v"(A[10]), "+v"(A[11]) :: "memory")
#define ADVANCE() { so += STRIDE; if (so >= HEAD_BYTES) so -= HEAD_BYTES; }
#define KSTEP(B, REFILL, RS, SLOT) { \
    float s0_ = 0.f, s1_ = 0.f, s2_ = 0.f; \
    _Pragma("unroll") for (int j_ = 0; j_ < 4; ++j_) { s0_ = dot2(B[0][j_], q[0][j_], s0_); s1_ = dot2(B[1][j_], q[1][j_], s1_); s2_ = dot2(B[2][j_], q[2][j_], s2_); } \
    asm volatile("" : "+v"(s0_), "+v"(s1_), "+v"(s2_) :: "memory"); \
    SB(); \
    if (REFILL) { _Pragma("unroll") for (int u_ = 0; u_ < 3; ++u_) B[u_] = LD128(RS, kvo + u_ * 128, wb + so, AUX_NT); ADVANCE(); } \
    SB(); \
    float s_ = s0_ + s1_ + s2_; \
    s_ += dpp<XOR1>(s_); s_ += dpp<XOR2>(s_); s_ += dpp<HMIR>(s_); \
    s_ = rbf(rbf(s_) * SCALE); \
    if (c == 0) sc[(SLOT) * 8] = s_; \
    MEMBAR(); \
    SB(); }
#define VSTEP(B, REFILL, P) { \
    f32x2 vf_[12]; \
    _Pragma("unroll") for (int t_ = 0; t_ < 3; ++t_) \
      _Pragma("unroll") for (int j_ = 0; j_ < 4; ++j_) vf_[t_ * 4 + j_] = bf2(B[t_][j_]); \
    PIN12(vf_); \
    SB(); \
    if (REFILL) { _Pragma("unroll") for (int u_ = 0; u_ < 3; ++u_) B[u_] = LD128(rv, kvo + u_ * 128, wb + so, AUX_NT); ADVANCE(); } \
    SB(); \
    _Pragma("unroll") for (int k_ = 0; k_ < 12; ++k_) acc[k_] = fma2(splat(P), vf_[k_], acc[k_]); \
    PIN12(acc); \
    SB(); }

    KSTEP(buf[0], 1, rk, 0);
    KSTEP(buf[1], 1, rk, 1);
#pragma unroll
    for (int jb = 2; jb < RPRE; ++jb) KSTEP(buf[jb], 0, rk, jb);
    sc += RPRE * 8;
#pragma nounroll
    for (int i = RPRE; i < STEPS - 2; i += 2) {
      if (i == WSTEPS - 2) wb = ROW1;
      if (i == WSTEPS) {
#pragma unroll
        for (int t = 0; t < 3; ++t) q[t] = *reinterpret_cast<const u32x4*>(ldu + L_Q + 96 + (8 * t + c) * 4);
      }
      KSTEP(buf[0], 1, rk, 0);
      KSTEP(buf[1], 1, rk, 1);
      sc += 16;
    }
    wb = 0;
    KSTEP(buf[0], 1, rv, 0);
    KSTEP(buf[1], 1, rv, 1);
    {
      f32x4* const sp4 = reinterpret_cast<f32x4*>(lds + L_SC + sw * 512) + lane;
      f32x4 sa = sp4[0], sb = sp4[64];
      float ma = fmaxf(fmaxf(sa.x, sa.y), fmaxf(sa.z, sa.w)), mb = fmaxf(fmaxf(sb.x, sb.y), fmaxf(sb.z, sb.w));
      ma = fmaxf(ma, dpp<XOR1>(ma)); ma = fmaxf(ma, dpp<XOR2>(ma)); ma = fmaxf(ma, dpp<HMIR>(ma)); ma = fmaxf(ma, dpp<ROR8>(ma));
      mb = fmaxf(mb, dpp<XOR1>(mb)); mb = fmaxf(mb, dpp<XOR2>(mb)); mb = fmaxf(mb, dpp<HMIR>(mb)); mb = fmaxf(mb, dpp<ROR8>(mb));
      ma = max3216(ma); mb = max3216(mb);
#pragma unroll
      for (int k = 0; k < 4; ++k) {
        sa[k] = __builtin_amdgcn_exp2f((sa[k] - ma) * LOG2E);
        sb[k] = __builtin_amdgcn_exp2f((sb[k] - mb) * LOG2E);
      }
      sp4[0] = sa; sp4[64] = sb;
      float l = fold32((sa.x + sa.y) + (sa.z + sa.w), (sb.x + sb.y) + (sb.z + sb.w));
      l = row_sum16(fold16(l, l));
      if ((lane & 31) == 0) *reinterpret_cast<f32x2*>(lds + L_ML + (lane >> 5) * 8 + 2 * sw) = f32x2{(lane ? mb : ma) * LOG2E, l};
      MEMBAR();
    }
    f32x2 acc[12];
#pragma unroll
    for (int k = 0; k < 12; ++k) acc[k] = f32x2{0.f, 0.f};
    sc = lds + L_SC + sw * 512 + r;
#define FLUSH(BB, ARR) { \
      float* const red_ = lds + L_RED + ((BB) * NSW + sw) * DH + fidx; \
      _Pragma("unroll") for (int t_ = 0; t_ < 3; ++t_) { \
        const float z0 = fold32(acc[t_ * 4 + 0].x, acc[t_ * 4 + 2].x), z1 = fold32(acc[t_ * 4 + 0].y, acc[t_ * 4 + 2].y); \
        const float z2 = fold32(acc[t_ * 4 + 1].x, acc[t_ * 4 + 3].x), z3 = fold32(acc[t_ * 4 + 1].y, acc[t_ * 4 + 3].y); \
        red_[64 * t_] = fold8(fold16(z0, z2), fold16(z1, z3), bit3); \
      } \
      MEMBAR(); \
      ARR = LDS_ARRIVE_FAST(BB); \
      MEMBAR(); }
#pragma nounroll
    for (int i = 0; i < STEPS - 2 - EB; i += 2) {
      const float p0 = sc[0], p1 = sc[8];
      sc += 16;
      if (i == WSTEPS - 2) wb = ROW1;
      if (i == TKI) { if (lane == 0) LDS_ARRIVE(9); }
      SB();
      VSTEP(buf[0], 1, p0);
      VSTEP(buf[1], 1, p1);
      if (i == WSTEPS - 2) {
        unsigned unused = 0;
        FLUSH(0, unused);
        (void)unused;
#pragma unroll
        for (int k = 0; k < 12; ++k) acc[k] = f32x2{0.f, 0.f};
      }
    }
#pragma unroll
    for (int jb = 2; jb < 2 + EB; ++jb) {
#pragma unroll
      for (int t = 0; t < 3; ++t) buf[jb][t] = LD128(rv, kvo + t * 128, wb + so, AUX_NT);
      ADVANCE();
      SB();
    }
    MEMBAR();
    LDS_WAIT(4, 1, 0);
    const f32x4 fw = *reinterpret_cast<const f32x4*>(lds + L_F);
    {
      float pl[2 + EB];
#pragma unroll
      for (int jb = 0; jb < 2 + EB; ++jb) pl[jb] = sc[8 * jb];
      MEMBAR();
      SB();
#pragma unroll
      for (int jb = 0; jb < 2 + EB; ++jb) VSTEP(buf[jb], 0, pl[jb]);
    }
    unsigned arrived = 0;
    FLUSH(1, arrived);
    f32x4 v[NSW];
    {
      const int d4 = lane < 48 ? 4 * lane : 0;
#pragma unroll
      for (int w = 0; w < NSW; ++w) v[w] = *reinterpret_cast<const f32x4*>(lds + L_RED + (NSW + w) * DH + d4);
      asm volatile("" : "+v"(arrived), "+v"(v[0]), "+v"(v[1]), "+v"(v[2]), "+v"(v[3]) :: "memory");
    }
    if (__builtin_amdgcn_readfirstlane(arrived) == NSW - 1) {
      const f32x4 o = (v[0] * fw.x + v[1] * fw.y) + (v[2] * fw.z + v[3] * fw.w);
      ST128((u32x4{vbit(o.x), vbit(o.y), vbit(o.z), vbit(o.w)}), rw, lane < 48 ? lane * 16 : OOB, WS_P + bid * 2048 + 1024);
      MEMBAR();
      ldu[L_CNT + 5] = 1u;
    }
    GET_UNIT(0);
    if (unit < NUNIT) {
      LDS_WAIT(11, NH, 0);
      wv = *reinterpret_cast<const f32x4*>(lds + L_W + 16 + ((wave >> 1) & 1) * 8 + (lane >> 5) * 4);
      LDS_WAIT(12 + sw, 1, 0);
#pragma unroll
      for (int i = 0; i < 6; ++i) {
        wop[i][0] = *reinterpret_cast<const u32x4*>(wl + (2 * i) * 256);
        wop[i][1] = *reinterpret_cast<const u32x4*>(wl + (2 * i + 1) * 256);
      }
      MEMBAR();
    }
    LDS_WAIT(5, 1, 0);
  } else {
    if (wave < 3) {
      const __amdgpu_buffer_rsrc_t rq = __builtin_amdgcn_make_buffer_rsrc((void*)wq, 0, D * H * DH * 2, RSRC3);
      unsigned xs[2][5], xp[2];
#pragma unroll
      for (int bb = 0; bb < 2; ++bb) {
#pragma unroll
        for (int i = 0; i < 5; ++i) xs[bb][i] = __builtin_amdgcn_raw_buffer_load_b32(rx, lane * 4 + i * 256 + bb * D * 2, 0, 0);
        xp[bb] = __builtin_amdgcn_raw_buffer_load_b32(rx, (c < 5 ? (8 * c + r) * 4 : 0) + bb * D * 2, qp * 160, 0);
      }
      const u32x2 ep = LD64(rw, epoch_off(lane), 0, AUX_SC1);
      SB();
      u32x4 wqr[5][2];
      {
        const unsigned vo = (unsigned)(2 * r) * (H * DH * 2) + c * 16;
        const unsigned qso = (unsigned)(80 * qp) * (H * DH * 2) + qh * (DH * 2) + wave * 128;
#pragma unroll
        for (int i = 0; i < 5; ++i)
#pragma unroll
          for (int e = 0; e < 2; ++e) { wqr[i][e] = LD128(rq, vo, qso + (16 * i + e) * (H * DH * 2), AUX_NT); SB(); }
      }
      float s0 = 0.f, s1 = 0.f, q0 = 0.f, q1 = 0.f;
#pragma unroll
      for (int i = 0; i < 5; ++i) {
        s0 = dot2(xs[0][i], ONES, s0); q0 = dot2(xs[0][i], xs[0][i], q0);
        s1 = dot2(xs[1][i], ONES, s1); q1 = dot2(xs[1][i], xs[1][i], q1);
      }
      const float st = row_sum16(fold16(fold32(s0, s1), fold32(q0, q1)));
      auto sw2 = __builtin_amdgcn_permlane16_swap(__float_as_uint(st), __float_as_uint(st), false, false);
      const float mean = __uint_as_float(sw2[0]) * (1.0f / D);
      const float var = fmaxf(fmaf(-mean, mean, __uint_as_float(sw2[1]) * (1.0f / D)), 0.f);
      const float inv = ln_den_inv(var), mb = rbf(mean);
      const float m0 = rdlane(mb, 0), m1 = rdlane(mb, 32), i0 = rdlane(inv, 0), i1 = rdlane(inv, 32);
      const unsigned xn0 = pack_bf16(rbf(bflo(xp[0]) - m0) * i0, rbf(bfhi(xp[0]) - m0) * i0);
      const unsigned xn1 = pack_bf16(rbf(bflo(xp[1]) - m1) * i1, rbf(bfhi(xp[1]) - m1) * i1);
      const unsigned long long tg = launch_tag(ep);
      const unsigned thi = (unsigned)(tg >> 32), t8 = (unsigned)tg & 0xff000000u;
      float a0[8], a1q[8];
#pragma unroll
      for (int k = 0; k < 8; ++k) { a0[k] = 0.f; a1q[k] = 0.f; }
#define QGROUP(I) { \
        unsigned b0_ = __builtin_amdgcn_update_dpp(0u, xn0, 0x150 + I, 0xf, 0x3, false); b0_ = __builtin_amdgcn_update_dpp(b0_, xn0, 0x158 + I, 0xf, 0xc, false); \
        unsigned b1_ = __builtin_amdgcn_update_dpp(0u, xn1, 0x150 + I, 0xf, 0x3, false); b1_ = __builtin_amdgcn_update_dpp(b1_, xn1, 0x158 + I, 0xf, 0xc, false); \
        _Pragma("unroll") for (int j = 0; j < 4; ++j) { \
          const unsigned plo = __builtin_amdgcn_perm(wqr[I][1][j], wqr[I][0][j], 0x05040100u), phi = __builtin_amdgcn_perm(wqr[I][1][j], wqr[I][0][j], 0x07060302u); \
          a0[2 * j] = dot2(plo, b0_, a0[2 * j]); a0[2 * j + 1] = dot2(phi, b0_, a0[2 * j + 1]); \
          a1q[2 * j] = dot2(plo, b1_, a1q[2 * j]); a1q[2 * j + 1] = dot2(phi, b1_, a1q[2 * j + 1]); } }
      QGROUP(0) QGROUP(1) QGROUP(2) QGROUP(3) QGROUP(4)
      float z[8];
#pragma unroll
      for (int k = 0; k < 8; ++k) z[k] = fold32(a0[k], a1q[k]);
      float y[4];
#pragma unroll
      for (int k = 0; k < 4; ++k) y[k] = fold16(z[k], z[k + 4]);
      const float u0 = fold8(y[0], y[2], bit3), u1 = fold8(y[1], y[3], bit3);
      const unsigned pidx = 32 * wave + 4 * c + 2 * ((lane >> 4) & 1) + (bit3 ? 1 : 0);
      ST128(pack2(u0, u1, t8, thi), rw, ((lane >> 5) * 8 * 96 + pidx) * 16, WS_QP + (qh * 16 + qp) * (96 * 16));
      asm volatile("s_waitcnt vmcnt(0)" ::: "memory");
      {
        const unsigned qo = ((lane >> 5) * 8 * 96 + 32 * wave + (lane & 31)) * 16;
        const unsigned qb = WS_QP + h * 16 * (96 * 16);
        u32x4 gq[8];
        for (unsigned spins = 0;; ++spins) {
          MEMBAR();
#pragma unroll
          for (int k = 0; k < 8; ++k) gq[k] = LD128(rw, qo, qb + k * (96 * 16), AUX_SC1);
          unsigned bad = 0;
#pragma unroll
          for (int k = 0; k < 8; ++k) bad |= tag_bad(gq[k], t8, thi);
          if (__all(bad == 0u) || spins > SPIN_CAP) break;
        }
        const f32x2 qq = ((g01(gq[0]) + g01(gq[2])) + (g01(gq[4]) + g01(gq[6]))) + ((g01(gq[1]) + g01(gq[3])) + (g01(gq[5]) + g01(gq[7])));
        ldu[L_Q + (lane >> 5) * 96 + 32 * wave + (lane & 31)] = pack_bf16(qq.x, qq.y);
      }
      if (tid == 0) *reinterpret_cast<u32x2*>(ldu + L_TAG) = u32x2{(unsigned)tg, thi};
    }
    const unsigned yoff = ((lane >> 5) * 8 * 320 + (lane & 31)) * 16;
    const unsigned ybo = WS_Y + (team * 320 + pc * 32) * 16 + b * 8;
    if (wave == 3) {
      if (lane < 16) ldu[L_CNT + lane] = 0u;
      const u32x2 ep = LD64(rw, epoch_off(lane), 0, AUX_SC1);
      ST128((u32x4{0u, 0u, 0u, 0u}), rw, lane * 16, WS_P + bid * 2048);
      ST128((u32x4{0u, 0u, 0u, 0u}), rw, lane * 16, WS_P + bid * 2048 + 1024);
      ST64((u32x2{0u, 0u}), rw, proj ? yoff : OOB, ybo);
      if (jj == 31) ST64((u32x2{0u, 0u}), rw, lane == 0 ? 0u : OOB, WS_TK + team * 128);
      asm volatile("s_waitcnt vmcnt(0)" ::: "memory");
      const unsigned long long tg = launch_tag(ep);
      ST64((u32x2{(unsigned)tg, (unsigned)(tg >> 32)}), rw, lane == 0 ? 0u : OOB, WS_RDY + (team * 32 + jj) * 8);
    }
    __syncthreads();
    LOAD_SMALL();
    if (wave == 0) {
      const u32x2 tg = *reinterpret_cast<const u32x2*>(ldu + L_TAG);
      for (unsigned spins = 0; spins < SPIN_CAP; ++spins) {
        __builtin_amdgcn_s_sleep(127);
        MEMBAR();
        const u32x4 r0 = LD128(rw, lane * 16, WS_RDY, AUX_SC1), r1 = LD128(rw, lane * 16 + 1024, WS_RDY, AUX_SC1);
        const unsigned bad = (r0.x ^ tg.x) | (r0.y ^ tg.y) | (r0.z ^ tg.x) | (r0.w ^ tg.y) | (r1.x ^ tg.x) | (r1.y ^ tg.y) | (r1.z ^ tg.x) | (r1.w ^ tg.y);
        if (__all(bad == 0u)) break;
      }
      LDS_WAIT(9, NSW, 4);
      unsigned tk = 0;
      if (lane == 0)
        tk = __hip_atomic_fetch_add((__attribute__((address_space(1))) unsigned*)(ws + WS_TK + team * 128), 1u, __ATOMIC_RELAXED, __HIP_MEMORY_SCOPE_AGENT);
      if (lane == 0) __hip_atomic_store(ldu + L_CNT + 10, tk + 1u, __ATOMIC_RELEASE, __HIP_MEMORY_SCOPE_WORKGROUP);
    }
    if (wave == 1) {
      LDS_WAIT(0, NSW, 16);
      const int d4 = lane < 48 ? 4 * lane : 0;
      const f32x4 ma = *reinterpret_cast<const f32x4*>(lds + L_ML + (lane >> 5) * 8), mc = *reinterpret_cast<const f32x4*>(lds + L_ML + (lane >> 5) * 8 + 4);
      f32x4 v[NSW];
#pragma unroll
      for (int w = 0; w < NSW; ++w) v[w] = *reinterpret_cast<const f32x4*>(lds + L_RED + w * DH + d4);
      const float M = __uint_as_float(vbit(fmaxf(fmaxf(ma.x, ma.z), fmaxf(mc.x, mc.z))));
      const f32x4 f = {__builtin_amdgcn_exp2f(ma.x - M), __builtin_amdgcn_exp2f(ma.z - M), __builtin_amdgcn_exp2f(mc.x - M), __builtin_amdgcn_exp2f(mc.z - M)};
      const float Ls = (ma.y * f.x + ma.w * f.y) + (mc.y * f.z + mc.w * f.w);
      if (lane == 32) *reinterpret_cast<f32x4*>(lds + L_F) = f;
      f32x4 o = (v[0] * rdlane(f.x, 0) + v[1] * rdlane(f.y, 0)) + (v[2] * rdlane(f.z, 0) + v[3] * rdlane(f.w, 0));
      const float M0 = rdlane(M, 0), L0 = rdlane(Ls, 0);
      if (lane >= 48) o = f32x4{M0, L0, M, Ls};
      ST128((u32x4{vbit(o.x), vbit(o.y), vbit(o.z), vbit(o.w)}), rw, lane <= 48 ? lane * 16 : OOB, WS_P + bid * 2048);
      if (lane == 0) LDS_ARRIVE(4);
    }
    GET_UNIT(4);
    if (unit < NUNIT) {
      {
        const __amdgpu_buffer_rsrc_t ro = __builtin_amdgcn_make_buffer_rsrc((void*)wo, 0, D * H * DH * 2, RSRC3);
        const unsigned vo = (unsigned)(2 * r) * (D * 2) + c * 16;
        const unsigned wso = (unsigned)(768 * team + 384 * (unit & 1) + 96 * wave) * (D * 2) + (unit >> 1) * 128;
        MEMBAR();
#pragma unroll
        for (int i = 0; i < 6; ++i) {
          wop[i][0] = LD128(ro, vo, wso + 16 * i * (D * 2), AUX_NT);
          wop[i][1] = LD128(ro, vo + D * 2, wso + 16 * i * (D * 2), AUX_NT);
        }
        MEMBAR();
        SB();
      }
      const unsigned mbase = WS_P + (unsigned)(team + 64 * (2 * (unit & 1) + (wave >> 1))) * 2048 + 48 * 16;
      u32x2 gm;
      for (unsigned spins = 0; spins < SPIN_CAP; ++spins) {
        MEMBAR();
        gm = LD64(rw, (lane & 7) * (8 * 2048) + (lane & 8), mbase, AUX_SC1);
        if (__all((gm.x & gm.y & 1u) != 0u)) break;
        __builtin_amdgcn_s_sleep(32);
      }
      const float ms = __uint_as_float(gm.x);
      float M = fmaxf(ms, dpp<XOR1>(ms)); M = fmaxf(M, dpp<XOR2>(M)); M = fmaxf(M, dpp<HMIR>(M));
      const float f = __builtin_amdgcn_exp2f(ms - M);
      float Ls = f * __uint_as_float(gm.y);
      Ls += dpp<XOR1>(Ls); Ls += dpp<XOR2>(Ls); Ls += dpp<HMIR>(Ls);
      if (lane < 16) lds[L_W + (lane >> 3) * 16 + (wave >> 1) * 8 + (lane & 1) * 4 + ((lane >> 1) & 3)] = f * __builtin_amdgcn_rcpf(Ls);
      MEMBAR();
      wv = *reinterpret_cast<const f32x4*>(lds + L_W + (wave >> 1) * 8 + (lane >> 5) * 4);
      if (lane == 0) LDS_ARRIVE(11);
    }
  }
  const u32x2 tg2 = *reinterpret_cast<const u32x2*>(ldu + L_TAG);
  const unsigned tlo = __builtin_amdgcn_readfirstlane(tg2.x), thi = __builtin_amdgcn_readfirstlane(tg2.y);

  const bool dproj = unit < NUNIT;
  const int rh = unit & 1, dpc = unit >> 1;
  if (dproj) {
    const unsigned pbase = WS_P + (unsigned)(team + 64 * (2 * rh + ((wave >> 1) & 1))) * 2048 + (wave >> 2) * 1024 + (wave & 1) * 384;
    const int j = lane & 31;
    const unsigned po = (lane >> 5) * (8 * 2048) + (j < 24 ? j : 0) * 16;
    u32x4 go[4];
    unsigned pend = 0xfu, spins = 0;
    do {
      MEMBAR();
#pragma unroll
      for (int k = 0; k < 4; ++k)
        if (pend & (1u << k)) go[k] = LD128(rw, po, pbase + k * (16 * 2048), AUX_SC1);
#pragma unroll
      for (int k = 0; k < 4; ++k)
        if (pend & (1u << k)) { if (__all((go[k].x & go[k].y & go[k].z & go[k].w & 1u) != 0u)) pend &= ~(1u << k); }
      if (wave < NH && pend != 0u) __builtin_amdgcn_s_sleep(32);
    } while (pend != 0u && ++spins < SPIN_CAP);
    const f32x4 o = (__builtin_bit_cast(f32x4, go[0]) * wv.x + __builtin_bit_cast(f32x4, go[1]) * wv.y) +
                    (__builtin_bit_cast(f32x4, go[2]) * wv.z + __builtin_bit_cast(f32x4, go[3]) * wv.w);
    const unsigned pr = pack_bf16(fold32(o.x, o.z), fold32(o.y, o.w));
    if (j < 24) ldu[L_O + 48 * wave + 2 * j + (lane >> 5)] = pr;
    MEMBAR();
  }
  if (dproj) {
    if (wave < NH) {
#pragma unroll
      for (int i = 0; i < 6; ++i) {
        const u32x4 e0 = wop[i][0], e1 = wop[i][1];
        u32x4 p0, p1;
        p0.x = __builtin_amdgcn_perm(e1.x, e0.x, 0x05040100u); p0.y = __builtin_amdgcn_perm(e1.x, e0.x, 0x07060302u);
        p0.z = __builtin_amdgcn_perm(e1.y, e0.y, 0x05040100u); p0.w = __builtin_amdgcn_perm(e1.y, e0.y, 0x07060302u);
        p1.x = __builtin_amdgcn_perm(e1.z, e0.z, 0x05040100u); p1.y = __builtin_amdgcn_perm(e1.z, e0.z, 0x07060302u);
        p1.z = __builtin_amdgcn_perm(e1.w, e0.w, 0x05040100u); p1.w = __builtin_amdgcn_perm(e1.w, e0.w, 0x07060302u);
        wop[i][0] = p0; wop[i][1] = p1;
        *reinterpret_cast<u32x4*>(wl + (2 * i) * 256) = p0;
        *reinterpret_cast<u32x4*>(wl + (2 * i + 1) * 256) = p1;
      }
      __hip_atomic_store(ldu + L_CNT + 12 + wave, 1u, __ATOMIC_RELEASE, __HIP_MEMORY_SCOPE_WORKGROUP);
    }
  }
  if (dproj) {
    unsigned op[6];
#pragma unroll
    for (int i = 0; i < 6; ++i) op[i] = ldu[L_O + 48 * wave + 8 * i + r];
    float y[8];
#pragma unroll
    for (int k = 0; k < 8; ++k) y[k] = 0.f;
#pragma unroll
    for (int i = 0; i < 6; ++i) {
#pragma unroll
      for (int k = 0; k < 4; ++k) { y[k] = dot2(wop[i][0][k], op[i], y[k]); y[k + 4] = dot2(wop[i][1][k], op[i], y[k + 4]); }
    }
    const float z0 = fold32(y[0], y[4]), z1 = fold32(y[1], y[5]), z2 = fold32(y[2], y[6]), z3 = fold32(y[3], y[7]);
    const float u = fold8(fold16(z0, z2), fold16(z1, z3), bit3);
    lds[L_YR + wave * 64 + fidx] = u;
    const int bb = wave >> 2;
    MEMBAR();
    unsigned arrived = LDS_ARRIVE_FAST(2 + bb);
    MEMBAR();
    f32x2 v[4];
#pragma unroll
    for (int w = 0; w < 4; ++w) v[w] = *reinterpret_cast<const f32x2*>(lds + L_YR + (4 * bb + w) * 64 + 2 * (lane & 31));
    asm volatile("" : "+v"(arrived), "+v"(v[0]), "+v"(v[1]), "+v"(v[2]), "+v"(v[3]) :: "memory");
    if (__builtin_amdgcn_readfirstlane(arrived) == 3) {
      const f32x2 ys = (v[0] + v[1]) + (v[2] + v[3]);
      ST64((u32x2{vbit(ys.x), vbit(ys.y)}), rw, lane < 32 ? lane * 16 : OOB, WS_Y + ((bb * 8 + team) * 320 + dpc * 32) * 16 + rh * 8);
    }
  }
  __syncthreads();
  CONV_SMALL();
  if (wave >= 3) {
    u32x4 gy[8];
    const unsigned yo = (tid - 192) * 16, ybase = WS_Y + b * (8 * 320 * 16);
    unsigned pend = 0xffu, spins = 0;
    do {
      MEMBAR();
#pragma unroll
      for (int s = 0; s < 8; ++s)
        if (pend & (1u << s)) gy[s] = LD128(rw, yo, ybase + s * (320 * 16), AUX_SC1);
#pragma unroll
      for (int s = 0; s < 8; ++s)
        if (pend & (1u << s)) { if (__all((gy[s].x & gy[s].y & gy[s].z & gy[s].w & 1u) != 0u)) pend &= ~(1u << s); }
    } while (pend != 0u && ++spins < SPIN_CAP);
    f32x2 ap[8];
#pragma unroll
    for (int s = 0; s < 8; ++s) ap[s] = asf2(gy[s].x, gy[s].y) + asf2(gy[s].z, gy[s].w);
    const f32x2 a = ((ap[0] + ap[1]) + (ap[2] + ap[3])) + ((ap[4] + ap[5]) + (ap[6] + ap[7]));
    const unsigned hp = pack_bf16(bflo(xres) + rbf(a.x), bfhi(xres) + rbf(a.y));
    ldu[L_H + tid - 192] = hp;
    float st = fold32(dot2(hp, ONES, 0.f), dot2(hp, hp, 0.f));
    st = row_sum16(fold16(st, st));
    if ((lane & 31) == 0) lds[L_ST + (wave - 3) * 2 + (lane >> 5)] = st;
  }
  __syncthreads();
  const int d0 = 5 * (team * 16 + cb);
  float he;
  {
    const int de = d0 + (lane < 5 ? lane : 0);
    const unsigned hw = ldu[L_H + (de >> 1)];
    f32x2 sv[5];
#pragma unroll
    for (int w = 0; w < 5; ++w) sv[w] = *reinterpret_cast<const f32x2*>(lds + L_ST + 2 * w);
    const f32x2 st = (((sv[0] + sv[1]) + (sv[2] + sv[3])) + sv[4]) * splat(1.0f / D);
    he = (de & 1) ? bfhi(hw) : bflo(hw);
    const float mean1 = st.x;
    const float var1 = fmaxf(fmaf(-mean1, mean1, st.y), 0.f);
    const float i1 = ln_den_inv(var1), m1 = rbf(mean1);
    const float m2 = rbf((mean1 - m1) * i1), i2 = ln_den_inv(var1 * i1 * i1);
    const float tl = rbf(rbf(rbf(rbf(he - m1) * i1) - m2) * i2);
    constexpr float G1 = -2.0f * 0.7978845608028654f * LOG2E, G3 = G1 * 0.044715f;
    float te[5];
#pragma unroll
    for (int e = 0; e < 5; ++e) te[e] = rdlane(tl, e);
    f32x2 u[8], z[8];
#pragma unroll
    for (int e = 0; e < 5; ++e) u[e] = fma2(splat(te[e]), fw1, fb1);
    u[5] = fma2(f32x2{te[0], te[1]}, splat(gw1), splat(gb1));
    u[6] = fma2(f32x2{te[2], te[3]}, splat(gw1), splat(gb1));
    u[7] = fma2(splat(te[4]), splat(gw1), splat(gb1));
    SB();
#pragma unroll
    for (int k = 0; k < 8; ++k) z[k] = u[k] * u[k];
    SB();
#pragma unroll
    for (int k = 0; k < 8; ++k) z[k] = fma2(z[k], splat(G3), splat(G1));
    SB();
#pragma unroll
    for (int k = 0; k < 8; ++k) z[k] = z[k] * u[k];
    SB();
#pragma unroll
    for (int k = 0; k < 8; ++k) { z[k].x = __builtin_amdgcn_exp2f(z[k].x); if (k < 7) z[k].y = __builtin_amdgcn_exp2f(z[k].y); }
#pragma unroll
    for (int k = 0; k < 8; ++k) u[k] = u[k] * (k < 5 ? fw2 : splat(gw2));
    SB();
#pragma unroll
    for (int k = 0; k < 8; ++k) z[k] = z[k] + splat(1.0f);
    SB();
#pragma unroll
    for (int k = 0; k < 8; ++k) { z[k].x = __builtin_amdgcn_rcpf(z[k].x); if (k < 7) z[k].y = __builtin_amdgcn_rcpf(z[k].y); }
    SB();
#pragma unroll
    for (int k = 0; k < 8; ++k) u[k] = u[k] * z[k];
    const float ya0 = (u[0].x + u[0].y) + u[5].x, ya1 = (u[1].x + u[1].y) + u[5].y, ya2 = (u[2].x + u[2].y) + u[6].x,
                ya3 = (u[3].x + u[3].y) + u[6].y, ya4 = (u[4].x + u[4].y) + u[7].x;
    const float f4 = fold32(ya4, ya4);
    float r0 = fold8(fold16(fold32(ya0, ya1), fold32(ya2, ya3)), fold16(f4, f4), bit3);
    r0 += dpp<XOR1>(r0); r0 += dpp<XOR2>(r0); r0 += dpp<HMIR>(r0);
    const int el = bit3 ? 4 : (lane >> 5) + 2 * ((lane >> 4) & 1);
    if ((lane & 15) == 0 || lane == 8) lds[L_MR + el * 8 + wave] = r0;
  }
  MEMBAR();
  unsigned arrived5 = LDS_ARRIVE_FAST(7);
  MEMBAR();
  f32x4 pa = *reinterpret_cast<const f32x4*>(lds + L_MR + (lane & 7) * 8), pb = *reinterpret_cast<const f32x4*>(lds + L_MR + (lane & 7) * 8 + 4);
  asm volatile("" : "+v"(arrived5), "+v"(pa), "+v"(pb) :: "memory");
  if (__builtin_amdgcn_readfirstlane(arrived5) == NWAVE - 1 && lane < 5) {
    const f32x4 ps = pa + pb;
    const float yv = (ps.x + ps.y) + (ps.z + ps.w);
    const unsigned o = pack_bf16(he + rbf(rbf(yv) + fb2), 0.f);
    __builtin_amdgcn_raw_buffer_store_b16((unsigned short)o, __builtin_amdgcn_make_buffer_rsrc((void*)out, 0, 2 * D * 2 + 4, RSRC3),
                                          lane * 2, (b * D + d0) * 2, 0);
  }
  if (tid < 4 && bid == 0) ST64((u32x2{tlo, thi}), rw, epoch_off(lane), 0);
}

}

extern "C" void kernel_launch(void* const* d_in, const int* in_sizes, int n_in, void* d_out, int out_size, void* d_ws,
                              size_t ws_size, hipStream_t stream) {
  (void)in_sizes; (void)n_in; (void)out_size; (void)ws_size;
  fused_kernel<<<dim3(NWG), dim3(NTHR), 0, stream>>>(
      (const char*)d_in[0], (const char*)d_in[1], (const char*)d_in[2], (const char*)d_in[3], (const char*)d_in[4],
      (const char*)d_in[6], (const char*)d_in[7], (const char*)d_in[8], (const char*)d_in[9], (char*)d_out, (char*)d_ws);
}
```
